```python
import torch

def _softmax(x, axis=-1):
    return torch.softmax(x, dim=axis)
def _gelu(x):
    return torch.nn.functional.gelu(x, approximate="tanh")
def _silu(x):
    return torch.nn.functional.silu(x)
def _sigmoid(x):
    return torch.sigmoid(x)
def _layernorm(x, axis=-1, eps=1e-5):
    m = x.mean(dim=axis, keepdim=True)
    v = x.var(dim=axis, keepdim=True, unbiased=False)
    return (x - m) / torch.sqrt(v + eps)
def _scatter_add(tgt, idx, src, axis=0):
    return tgt.clone().index_add_(axis, idx, src)
def _segment_sum(src, seg, n):
    out = torch.zeros((n,) + src.shape[1:], dtype=src.dtype)
    return out.index_add_(0, seg, src)
def _pad(x, pw):
    flat = tuple(v for pair in reversed(pw) for v in pair)
    return torch.nn.functional.pad(x, flat)

kernel_name = 'sequential_preLN__gqa_block_cached__geglu_ffn__decode'

def setup_inputs():

    _w = (lambda a: (lambda t: t.to(torch.bfloat16) if t.is_floating_point() else t)(torch.as_tensor(a)))
    def _A_setup():
        B, Sq, D = 8, 1, 512
        Hq, Hkv, Sk, Dh = 32, 8, 8192, 128
        assert Hq % Hkv == 0, (Hq, Hkv)
        return {
            "x": _w((lambda *s: torch.randn(*s, dtype=torch.bfloat16) * 0.05)(B, Sq, D)),
            "k_cache": _w((lambda *s: torch.randn(*s, dtype=torch.bfloat16) * 3.2)(B, Hkv, Sk, Dh)),
            "v_cache": _w((lambda *s: torch.randn(*s, dtype=torch.bfloat16) * 0.4)(B, Hkv, Sk, Dh)),
            "wq": _w((lambda *s: torch.randn(*s, dtype=torch.bfloat16) * 3.2)(D, Hq * Dh) * (D ** -0.5)),
            "wo": _w((lambda *s: torch.randn(*s, dtype=torch.bfloat16) * 0.4)(Hq * Dh, D) * ((Hq * Dh) ** -0.5)),
        }
    _A_in = _A_setup()

    def _B_setup():
        B, S, D, F = 8, 1, 512, 1792
        return {
            "x": _w((lambda *s: torch.randn(*s, dtype=torch.bfloat16) * 0.05)(B, S, D)),
            "w_gate": _w((lambda *s: torch.randn(*s, dtype=torch.bfloat16) * 0.05)(D, F)),
            "w_up": _w((lambda *s: torch.randn(*s, dtype=torch.bfloat16) * 0.05)(D, F)),
            "w_down": _w((lambda *s: torch.randn(*s, dtype=torch.bfloat16) * 0.05)(F, D)),
        }
    _B_in = _B_setup()

    return {
        "A_x": _A_in["x"],
        "A_k_cache": _A_in["k_cache"],
        "A_v_cache": _A_in["v_cache"],
        "A_wq": _A_in["wq"],
        "A_wo": _A_in["wo"],
        "B_x": _B_in["x"],
        "B_w_gate": _B_in["w_gate"],
        "B_w_up": _B_in["w_up"],
        "B_w_down": _B_in["w_down"],
    }

def reference(A_x, A_k_cache, A_v_cache, A_wq, A_wo, B_x, B_w_gate, B_w_up, B_w_down):
    def _A_ref(x, k_cache, v_cache, wq, wo):
        B, Sq, D = x.shape
        Hq, Dh = 32, 128
        Hkv = k_cache.shape[1]
        rep = Hq // Hkv
        q = (lambda x, *d: x.permute(*d))((x @ wq).reshape(B, Sq, Hq, Dh), 0, 2, 1, 3)
        k = (lambda x, n, axis: torch.repeat_interleave(x, n, dim=axis))(k_cache, rep, axis=1)
        v = (lambda x, n, axis: torch.repeat_interleave(x, n, dim=axis))(v_cache, rep, axis=1)
        scores = torch.einsum("bhqd,bhkd->bhqk", q, k) * (Dh ** -0.5)
        p = _softmax(scores, axis=-1)
        o = torch.einsum("bhqk,bhkd->bhqd", p, v)
        return (lambda x, *d: x.permute(*d))(o, 0, 2, 1, 3).reshape(B, Sq, Hq * Dh) @ wo

    def _B_ref(x, w_gate, w_up, w_down):
        return (_gelu(x @ w_gate) * (x @ w_up)) @ w_down

    x = A_x
    h = x + _A_ref(_layernorm(x, axis=-1), A_k_cache, A_v_cache, A_wq, A_wo)
    h2 = h + _B_ref(_layernorm(h, axis=-1), B_w_gate, B_w_up, B_w_down)
    return h2
```

```cpp
#include <hip/hip_runtime.h>
#include <hip/hip_bf16.h>

namespace {

#define LAS __attribute__((address_space(3)))
#define GAS __attribute__((address_space(1)))
typedef unsigned u32x4 __attribute__((ext_vector_type(4)));
typedef unsigned u32x2 __attribute__((ext_vector_type(2)));
typedef float f32x4 __attribute__((ext_vector_type(4)));
typedef float f32x2 __attribute__((ext_vector_type(2)));
typedef short s16x4 __attribute__((ext_vector_type(4)));
typedef __bf16 bf16x8 __attribute__((ext_vector_type(8)));
typedef __bf16 bf16x2 __attribute__((ext_vector_type(2)));
typedef __amdgpu_buffer_rsrc_t rsrc_t;

constexpr int NB = 8, DM = 512, HQ = 32, HKV = 8, SK = 8192, DH = 128, FF = 1792;
constexpr int NWG = 256, NTHR = 512;
constexpr int NFFN = FF / 16;
constexpr float SCALE = 0.08838834764831845f, LOG2E = 1.4426950408889634f, EPS = 1e-5f;

#define RING_W 24
#define RING_W0 6
#define RING_W1 8
constexpr int W = RING_W, W0 = RING_W0, W1 = RING_W1;
constexpr unsigned SPIN_CAP = 4000;

constexpr int WS_GEN = 0;
constexpr int WS_Q = 4096;
constexpr int WS_O = WS_Q + NB * HKV * 256 * 8;
constexpr int WS_ML = WS_O + 64 * 8 * 4 * 64 * 8;
constexpr int WS_Y = WS_ML + 32 * 8 * 4 * 16;
constexpr int WS_H = WS_Y + 256 * 64 * 128;
constexpr int WS_D = WS_H + 8 * 256 * 8;
constexpr int WS_END = WS_D + 256 * NFFN * 128;

constexpr int L_WQ = 0;
constexpr int L_WO = 16384;
constexpr int L_WG = 32768;
constexpr int L_WU = 49152;
constexpr int L_WD = 65536;
constexpr int XN_STRIDE = 1040;
constexpr int L_XN = 81920;
constexpr int L_RED = L_XN + 8 * XN_STRIDE;
constexpr int L_ML = L_RED + 16384;
constexpr int L_OB = L_ML + 256;
constexpr int L_END = L_OB + 8 * 144;
static_assert(L_END <= 163840 && L_END > 81920, "one workgroup per CU");

struct Params {
  const unsigned char *x, *k, *v, *wq, *wo, *wg, *wu, *wd;
  unsigned char *out, *ws;
};

constexpr int AUX_NT = 2, AUX_SC1 = 16;
constexpr int RS3 = 0x00020000;

__device__ __forceinline__ rsrc_t mkrs(const void* p, int bytes) { return __builtin_amdgcn_make_buffer_rsrc((void*)p, 0, bytes, RS3); }
__device__ __forceinline__ float asf(unsigned u) { return __builtin_bit_cast(float, u); }
__device__ __forceinline__ unsigned asu(float f) { return __builtin_bit_cast(unsigned, f); }
__device__ __forceinline__ unsigned cvtpk(float lo, float hi) { f32x2 f = {lo, hi}; return __builtin_bit_cast(unsigned, __builtin_convertvector(f, bf16x2)); }
__device__ __forceinline__ float blo(unsigned u) { return asf(u << 16); }
__device__ __forceinline__ float bhi(unsigned u) { return asf(u & 0xffff0000u); }
__device__ __forceinline__ float bfr(float f) { return blo(cvtpk(f, f)); }
template <int C> __device__ __forceinline__ float dpp(float x) { return asf(__builtin_amdgcn_mov_dpp(asu(x), C, 0xf, 0xf, true)); }
template <int C> __device__ __forceinline__ unsigned dppu(unsigned x) { return __builtin_amdgcn_mov_dpp(x, C, 0xf, 0xf, true); }
constexpr int XOR1 = 0xB1, XOR2 = 0x4E, HMIRR = 0x141, MIRR = 0x140, ROR4 = 0x124, ROR8 = 0x128, ROR12 = 0x12C;
__device__ __forceinline__ float xrow_sum(float x) {
  auto s = __builtin_amdgcn_permlane16_swap(asu(x), asu(x), false, false);
  x = asf(s[0]) + asf(s[1]);
  auto t = __builtin_amdgcn_permlane32_swap(asu(x), asu(x), false, false);
  return asf(t[0]) + asf(t[1]);
}
__device__ __forceinline__ float xrow_max(float x) {
  auto s = __builtin_amdgcn_permlane16_swap(asu(x), asu(x), false, false);
  x = fmaxf(asf(s[0]), asf(s[1]));
  auto t = __builtin_amdgcn_permlane32_swap(asu(x), asu(x), false, false);
  return fmaxf(asf(t[0]), asf(t[1]));
}
__device__ __forceinline__ float wave_sum(float x) {
  x += dpp<XOR1>(x); x += dpp<XOR2>(x); x += dpp<HMIRR>(x); x += dpp<MIRR>(x);
  return xrow_sum(x);
}
__device__ __forceinline__ f32x4 mfma32(u32x4 a, u32x4 b, f32x4 c) {
  return __builtin_amdgcn_mfma_f32_16x16x32_bf16(__builtin_bit_cast(bf16x8, a), __builtin_bit_cast(bf16x8, b), c, 0, 0, 0);
}
__device__ __forceinline__ f32x4 mfma16(u32x2 a, u32x2 b, f32x4 c) {
  return __builtin_amdgcn_mfma_f32_16x16x16bf16_1k(__builtin_bit_cast(s16x4, a), __builtin_bit_cast(s16x4, b), c, 0, 0, 0);
}
__device__ __forceinline__ u32x2 trread(LAS unsigned char* p) { return __builtin_bit_cast(u32x2, __builtin_amdgcn_ds_read_tr16_b64_v4i16((LAS s16x4*)p)); }
__device__ __forceinline__ void wg_barrier() {
  asm volatile("s_waitcnt lgkmcnt(0)" ::: "memory");
  __builtin_amdgcn_s_barrier();
  asm volatile("" ::: "memory");
}
__device__ __forceinline__ void dma16(rsrc_t rs, unsigned voff, unsigned soff, unsigned ldsaddr) {
  unsigned keep;
  asm volatile("s_nop 4\n\ts_mov_b32 %0, m0\n\ts_mov_b32 m0, %1\n\ts_nop 0\n\tbuffer_load_dwordx4 %2, %3, %4 offen nt lds\n\ts_mov_b32 m0, %0"
               : "=&s"(keep) : "s"(ldsaddr), "v"(voff), "s"(rs), "s"(soff) : "memory");
}
__device__ __forceinline__ bool all_lanes(bool ok) { return __builtin_amdgcn_ballot_w64(!ok) == 0ull; }
#define SPIN_STEP() { asm volatile("" ::: "memory"); __builtin_amdgcn_s_sleep(1); if (++spins > SPIN_CAP) { failed = true; break; } }

__device__ __forceinline__ u32x4 layernorm8(const float (&v)[8]) {
  float s = ((v[0] + v[1]) + (v[2] + v[3])) + ((v[4] + v[5]) + (v[6] + v[7]));
  const float mean = wave_sum(s) * (1.0f / DM);
  float q = 0.f;
#pragma unroll
  for (int j = 0; j < 8; ++j) { const float d = v[j] - mean; q = fmaf(d, d, q); }
  const float var = bfr(wave_sum(q) * (1.0f / DM));
  const float mb = bfr(mean);
  const float den = bfr(__builtin_amdgcn_sqrtf(bfr(var + EPS)));
  const float r = __builtin_amdgcn_rcpf(den);
  u32x4 o;
#pragma unroll
  for (int j = 0; j < 4; ++j) {
    const unsigned d = cvtpk(v[2 * j] - mb, v[2 * j + 1] - mb);
    o[j] = cvtpk(blo(d) * r, bhi(d) * r);
  }
  return o;
}

__global__ void __launch_bounds__(NTHR, 2) fused_decode(Params p) {
  __shared__ __attribute__((aligned(16))) unsigned char smem[L_END];
  LAS unsigned char* L = (LAS unsigned char*)smem;
  const unsigned lbase = (unsigned)(size_t)L;
  const int tid = threadIdx.x, lane = tid & 63, w = __builtin_amdgcn_readfirstlane(tid >> 6);
  const int bid = blockIdx.x, xl = bid & 7, yl = bid >> 3;
  const int q4 = lane >> 4, c16 = lane & 15;
  const rsrc_t rws = mkrs(p.ws, WS_END);
  bool failed = false;

  const unsigned gen = __hip_atomic_load((GAS unsigned*)(p.ws + WS_GEN), __ATOMIC_RELAXED, __HIP_MEMORY_SCOPE_AGENT);
  const u32x4 xrow = *(const GAS u32x4*)(p.x + w * (DM * 2) + lane * 16);
  {
    const rsrc_t rq = mkrs(p.wq, DM * HQ * DH * 2);
    const unsigned vo = (unsigned)((64 * w + (lane >> 1)) * (HQ * DH * 2) + (32 * xl + yl) * 32 + (lane & 1) * 16);
    dma16(rq, vo, 0u, lbase + L_WQ + w * 2048);
    dma16(rq, vo, 32u * (HQ * DH * 2), lbase + L_WQ + w * 2048 + 1024);
  }
  const int ab = yl >> 2, ag = xl, as = yl & 3;
  const rsrc_t rk = mkrs(p.k + ((size_t)(ab * HKV + ag) * SK + as * 2048) * (DH * 2), 2048 * DH * 2);
  const rsrc_t rv = mkrs(p.v + ((size_t)(ab * HKV + ag) * SK + as * 2048) * (DH * 2), 2048 * DH * 2);
  const int vk = w * 4096 + ((lane >> 2) & 3) * 256 + (q4 * 4 + (lane & 3)) * 16;
  const int vv = w * 4096 + lane * 16;
  u32x4 ring[W];
#define LDN(n) ((n) < 64 ? __builtin_amdgcn_raw_buffer_load_b128(rk, vk + ((n) & 3) * 1024, ((n) >> 2) * 32768, AUX_NT) \
                         : __builtin_amdgcn_raw_buffer_load_b128(rv, vv + ((n) & 3) * 1024, (((n) - 64) >> 2) * 32768, AUX_NT))
#pragma unroll
  for (int i = 0; i < W0; ++i) ring[i] = LDN(i);
  __builtin_amdgcn_sched_barrier(0);

  {
    float xv[8];
#pragma unroll
    for (int j = 0; j < 4; ++j) { xv[2 * j] = blo(xrow[j]); xv[2 * j + 1] = bhi(xrow[j]); }
    *(LAS u32x4*)(L + L_XN + w * XN_STRIDE + lane * 16) = layernorm8(xv);
  }
  asm volatile("s_waitcnt vmcnt(%0)" ::"i"(W0) : "memory");
  wg_barrier();
  const unsigned tag = __builtin_amdgcn_readfirstlane(gen) + 1u;
  {
    f32x4 acc = {0.f, 0.f, 0.f, 0.f};
#pragma unroll
    for (int t = 0; t < 2; ++t) {
      const int k0 = 64 * w + 32 * t;
      LAS unsigned char* pa = L + L_WQ + (k0 + 8 * q4 + (c16 >> 2)) * 32 + (c16 & 3) * 8;
      const u32x2 a0 = trread(pa), a1 = trread(pa + 128);
      const u32x4 bx = *(LAS u32x4*)(L + L_XN + (c16 & 7) * XN_STRIDE + (k0 + 8 * q4) * 2);
      acc = mfma32(u32x4{a0.x, a0.y, a1.x, a1.y}, bx, acc);
    }
    *(LAS f32x4*)(L + L_RED + w * 1024 + lane * 16) = acc;
  }
  wg_barrier();
  if (w == 0) {
    const int b = lane >> 3, np = lane & 7;
    float s0 = 0.f, s1 = 0.f;
#pragma unroll
    for (int ww = 0; ww < 8; ++ww) {
      const f32x2 v = *(LAS f32x2*)(L + L_RED + ww * 1024 + ((np >> 1) * 16 + b) * 16 + (np & 1) * 8);
      s0 += v.x; s1 += v.y;
    }
    __builtin_amdgcn_raw_buffer_store_b64(u32x2{cvtpk(s0, s1), tag}, rws, WS_Q + ((b * 8 + xl) * 256 + 8 * yl + np) * 8, 0, AUX_SC1);
  }
  __builtin_amdgcn_sched_barrier(0);
  const bool ffn = (xl & 1) && yl < 28;
  const int pf = (xl >> 1) * 28 + yl;
  const int kt = bid >> 2, nt = bid & 3;
  {
    const rsrc_t ro = mkrs(p.wo, HQ * DH * DM * 2);
    const unsigned vo = (unsigned)((kt * 64 + 8 * w + q4) * (DM * 2) + nt * 256 + c16 * 16);
    dma16(ro, vo, 0u, lbase + L_WO + w * 2048);
    dma16(ro, vo, 4u * (DM * 2), lbase + L_WO + w * 2048 + 1024);
    if (ffn) {
      const rsrc_t rg = mkrs(p.wg, DM * FF * 2), ru = mkrs(p.wu, DM * FF * 2), rd = mkrs(p.wd, FF * DM * 2);
      const unsigned vg = (unsigned)((64 * w + (lane >> 1)) * (FF * 2) + pf * 32 + (lane & 1) * 16);
      dma16(rg, vg, 0u, lbase + L_WG + w * 2048);
      dma16(rg, vg, 32u * (FF * 2), lbase + L_WG + w * 2048 + 1024);
      dma16(ru, vg, 0u, lbase + L_WU + w * 2048);
      dma16(ru, vg, 32u * (FF * 2), lbase + L_WU + w * 2048 + 1024);
      const unsigned vd = (unsigned)((pf * 16 + 2 * w) * (DM * 2) + lane * 16);
      dma16(rd, vd, 0u, lbase + L_WD + w * 2048);
      dma16(rd, vd, (unsigned)(DM * 2), lbase + L_WD + w * 2048 + 1024);
    }
  }
#pragma unroll
  for (int i = W0; i < W0 + W1; ++i) ring[i] = LDN(i);
  u32x4 qf;
  {
    const int qo = WS_Q + (ab * 8 + ag) * 2048 + (lane & 3) * 512 + q4 * 128 + ((lane >> 2) & 3) * 32;
    u32x4 g0 = __builtin_amdgcn_raw_buffer_load_b128(rws, qo, 0, AUX_SC1), g1 = __builtin_amdgcn_raw_buffer_load_b128(rws, qo + 16, 0, AUX_SC1);
#pragma unroll
    for (int i = W0 + W1; i < W; ++i) ring[i] = LDN(i);
    __builtin_amdgcn_sched_barrier(0);
    if (!all_lanes(g0.y == tag && g0.w == tag && g1.y == tag && g1.w == tag)) {
      for (unsigned spins = 0;;) {
        SPIN_STEP();
        g0 = __builtin_amdgcn_raw_buffer_load_b128(rws, qo, 0, AUX_SC1); g1 = __builtin_amdgcn_raw_buffer_load_b128(rws, qo + 16, 0, AUX_SC1);
        if (all_lanes(g0.y == tag && g0.w == tag && g1.y == tag && g1.w == tag)) break;
      }
    }
    qf = u32x4{g0.x, g0.z, g1.x, g1.z};
  }

  float T[16];
#pragma unroll
  for (int i = 0; i < 64; ++i) {
    const f32x4 d = mfma32(ring[i % W], qf, f32x4{0.f, 0.f, 0.f, 0.f});
    ring[i % W] = LDN(i + W);
    const int x = (i & 7) >> 1, r = 2 * (i >> 3) + (i & 1);
    const float s = ((d[x] + dpp<ROR12>(d[(x + 1) & 3])) + dpp<ROR8>(d[(x + 2) & 3])) + dpp<ROR4>(d[(x + 3) & 3]);
    if (x == 0) T[r] = s;
    else if (x == 1) T[r] = asf(__builtin_amdgcn_update_dpp(asu(T[r]), asu(s), 0xE4, 0xf, 0x2, false));
    else if (x == 2) T[r] = asf(__builtin_amdgcn_update_dpp(asu(T[r]), asu(s), 0xE4, 0xf, 0x4, false));
    else T[r] = asf(__builtin_amdgcn_update_dpp(asu(T[r]), asu(s), 0xE4, 0xf, 0x8, false));
    __builtin_amdgcn_sched_barrier(0);
  }
  float m_w, l_w;
  unsigned P[8];
  {
    float mx = -INFINITY;
#pragma unroll
    for (int t = 0; t < 8; ++t) {
      const unsigned u = cvtpk(T[2 * t], T[2 * t + 1]);
      const unsigned u2 = cvtpk(blo(u) * SCALE, bhi(u) * SCALE);
      T[2 * t] = blo(u2); T[2 * t + 1] = bhi(u2);
      mx = fmaxf(mx, fmaxf(T[2 * t], T[2 * t + 1]));
    }
    mx = fmaxf(mx, dpp<ROR4>(mx)); mx = fmaxf(mx, dpp<ROR8>(mx)); mx = xrow_max(mx);
    float ls = 0.f;
#pragma unroll
    for (int t = 0; t < 8; ++t) {
      const float a = __builtin_amdgcn_exp2f((T[2 * t] - mx) * LOG2E), b = __builtin_amdgcn_exp2f((T[2 * t + 1] - mx) * LOG2E);
      ls += a + b;
      P[t] = cvtpk(a, b);
    }
    ls += dpp<ROR4>(ls); ls += dpp<ROR8>(ls);
    l_w = xrow_sum(ls); m_w = mx;
  }
  f32x4 acc[8];
#pragma unroll
  for (int j = 0; j < 8; ++j) acc[j] = f32x4{0.f, 0.f, 0.f, 0.f};
#pragma unroll
  for (int t = 0; t < 8; ++t) {
    u32x4 bf[8];
#pragma unroll
    for (int dw = 0; dw < 4; ++dw) {
      const int n0 = 64 + 8 * t + 2 * dw, n1 = n0 + 1;
      const u32x4 a = ring[n0 % W], b = ring[n1 % W];
#pragma unroll
      for (int e = 0; e < 4; ++e) {
        bf[2 * e][dw] = __builtin_amdgcn_perm(b[e], a[e], 0x05040100u);
        bf[2 * e + 1][dw] = __builtin_amdgcn_perm(b[e], a[e], 0x07060302u);
      }
      if (n0 + W < 128) ring[n0 % W] = LDN(n0 + W);
      if (n1 + W < 128) ring[n1 % W] = LDN(n1 + W);
      __builtin_amdgcn_sched_barrier(0);
    }
    const u32x4 af = {P[t], dppu<ROR12>(P[t]), dppu<ROR8>(P[t]), dppu<ROR4>(P[t])};
#pragma unroll
    for (int j = 0; j < 8; ++j) acc[j] = mfma32(af, bf[j], acc[j]);
    __builtin_amdgcn_sched_barrier(0);
  }
  if (lane < 16) {
#pragma unroll
    for (int h = 0; h < 4; ++h) {
      LAS f32x4* dst = (LAS f32x4*)(L + L_WQ + w * 2048 + h * 512 + lane * 32);
      dst[0] = f32x4{acc[0][h], acc[1][h], acc[2][h], acc[3][h]};
      dst[1] = f32x4{acc[4][h], acc[5][h], acc[6][h], acc[7][h]};
    }
    if (lane < 4) *(LAS f32x2*)(L + L_ML + (w * 4 + lane) * 8) = f32x2{m_w, l_w};
  }
  asm volatile("s_waitcnt vmcnt(0)" ::: "memory");
  wg_barrier();
  {
    const int h = tid >> 7, d = tid & 127;
    float mw[8], lw[8], M = -INFINITY;
#pragma unroll
    for (int ww = 0; ww < 8; ++ww) { const f32x2 v = *(LAS f32x2*)(L + L_ML + (ww * 4 + h) * 8); mw[ww] = v.x; lw[ww] = v.y; M = fmaxf(M, v.x); }
    float O = 0.f, Ls = 0.f;
#pragma unroll
    for (int ww = 0; ww < 8; ++ww) {
      const float e = __builtin_amdgcn_exp2f((mw[ww] - M) * LOG2E);
      O = fmaf(e, *(LAS float*)(L + L_WQ + ww * 2048 + h * 512 + d * 4), O);
      Ls = fmaf(e, lw[ww], Ls);
    }
    const int hg = ag * 4 + h;
    __builtin_amdgcn_raw_buffer_store_b64(u32x2{asu(O), tag}, rws, WS_O + ((((hg * 2 + (d >> 6)) * 8 + ab) * 4 + as) * 64 + (d & 63)) * 8, 0, AUX_SC1);
    if (d == 0) __builtin_amdgcn_raw_buffer_store_b128(u32x4{asu(M), tag, asu(Ls), tag}, rws, WS_ML + ((hg * 8 + ab) * 4 + as) * 16, 0, AUX_SC1);
  }
  u32x4 wof[2], wgf[2], wuf[2];
  u32x2 wdf[4];
#pragma unroll
  for (int t = 0; t < 2; ++t) {
    LAS unsigned char* pa = L + L_WO + (32 * t + 8 * q4 + (c16 >> 2)) * 256 + w * 32 + (c16 & 3) * 8;
    const u32x2 a0 = trread(pa), a1 = trread(pa + 4 * 256);
    wof[t] = u32x4{a0.x, a0.y, a1.x, a1.y};
    LAS unsigned char* pg = L + L_WG + (64 * w + 32 * t + 8 * q4 + (c16 >> 2)) * 32 + (c16 & 3) * 8;
    const u32x2 g0 = trread(pg), g1 = trread(pg + 128), u0 = trread(pg + (L_WU - L_WG)), u1 = trread(pg + (L_WU - L_WG) + 128);
    wgf[t] = u32x4{g0.x, g0.y, g1.x, g1.y};
    wuf[t] = u32x4{u0.x, u0.y, u1.x, u1.y};
  }
#pragma unroll
  for (int j = 0; j < 4; ++j) wdf[j] = trread(L + L_WD + (4 * q4 + (c16 >> 2)) * 1024 + (64 * w + 16 * j) * 2 + (c16 & 3) * 8);
  const int rb = 4 * (bid >> 7) + ((tid & 7) >> 1), rc = 4 * (bid & 127) + 2 * (tid & 1);
  const unsigned xres = *(const GAS unsigned*)(p.x + (rb * DM + rc) * 2);

  {
    const int oo = WS_O + ((kt * 8 + w) * 4) * 512 + lane * 8, mo = WS_ML + (((kt >> 1) * 8 + w) * 4) * 16;
    u32x2 og[4]; u32x4 ml[4];
    for (unsigned spins = 0;;) {
      bool ok = true;
#pragma unroll
      for (int s = 0; s < 4; ++s) {
        og[s] = __builtin_amdgcn_raw_buffer_load_b64(rws, oo + s * 512, 0, AUX_SC1);
        ml[s] = __builtin_amdgcn_raw_buffer_load_b128(rws, mo + s * 16, 0, AUX_SC1);
      }
#pragma unroll
      for (int s = 0; s < 4; ++s) ok = ok && og[s].y == tag && ml[s].y == tag && ml[s].w == tag;
      if (all_lanes(ok)) break;
      SPIN_STEP();
    }
    const float M = fmaxf(fmaxf(asf(ml[0].x), asf(ml[1].x)), fmaxf(asf(ml[2].x), asf(ml[3].x)));
    float O = 0.f, Ls = 0.f;
#pragma unroll
    for (int s = 0; s < 4; ++s) {
      const float e = __builtin_amdgcn_exp2f((asf(ml[s].x) - M) * LOG2E);
      O = fmaf(e, asf(og[s].x), O); Ls = fmaf(e, asf(ml[s].z), Ls);
    }
    const unsigned ob = cvtpk(O * __builtin_amdgcn_rcpf(Ls), 0.f);
    *(LAS unsigned short*)(L + L_OB + w * 144 + lane * 2) = (unsigned short)ob;
    wg_barrier();
    f32x4 y = {0.f, 0.f, 0.f, 0.f};
#pragma unroll
    for (int t = 0; t < 2; ++t) y = mfma32(wof[t], *(LAS u32x4*)(L + L_OB + (c16 & 7) * 144 + (32 * t + 8 * q4) * 2), y);
    if (c16 < 8) {
      const int cg = nt * 32 + w * 4 + q4, r = (c16 >> 2) * 128 + cg;
      const int yo = WS_Y + (r * 64 + kt) * 128 + (c16 & 3) * 32;
      __builtin_amdgcn_raw_buffer_store_b128(u32x4{asu(y[0]), tag, asu(y[1]), tag}, rws, yo, 0, AUX_SC1);
      __builtin_amdgcn_raw_buffer_store_b128(u32x4{asu(y[2]), tag, asu(y[3]), tag}, rws, yo + 16, 0, AUX_SC1);
    }
  }
  unsigned hres = 0;
  {
    const int yo = WS_Y + bid * (64 * 128) + tid * 16;
    u32x4 g;
    for (unsigned spins = 0;;) {
      g = __builtin_amdgcn_raw_buffer_load_b128(rws, yo, 0, AUX_SC1);
      if (failed || all_lanes(g.y == tag && g.w == tag)) break;
      SPIN_STEP();
    }
    float a = asf(g.x), b = asf(g.z);
    a += dpp<ROR8>(a); b += dpp<ROR8>(b);
    a = xrow_sum(a); b = xrow_sum(b);
    if (lane < 8) *(LAS f32x2*)(L + L_RED + (w * 8 + lane) * 8) = f32x2{a, b};
    wg_barrier();
    if (tid < 8) {
      float s0 = 0.f, s1 = 0.f;
#pragma unroll
      for (int ww = 0; ww < 8; ++ww) { const f32x2 v = *(LAS f32x2*)(L + L_RED + (ww * 8 + tid) * 8); s0 += v.x; s1 += v.y; }
      const unsigned at = cvtpk(s0, s1);
      hres = cvtpk(blo(xres) + blo(at), bhi(xres) + bhi(at));
      __builtin_amdgcn_raw_buffer_store_b64(u32x2{hres, tag}, rws, WS_H + (rb * 256 + (rc >> 1)) * 8, 0, AUX_SC1);
    }
  }
  if (ffn) {
    const int ho = WS_H + w * 2048 + lane * 32;
    u32x4 g0, g1;
    for (unsigned spins = 0;;) {
      g0 = __builtin_amdgcn_raw_buffer_load_b128(rws, ho, 0, AUX_SC1); g1 = __builtin_amdgcn_raw_buffer_load_b128(rws, ho + 16, 0, AUX_SC1);
      if (failed || all_lanes(g0.y == tag && g0.w == tag && g1.y == tag && g1.w == tag)) break;
      SPIN_STEP();
    }
    const float hv[8] = {blo(g0.x), bhi(g0.x), blo(g0.z), bhi(g0.z), blo(g1.x), bhi(g1.x), blo(g1.z), bhi(g1.z)};
    *(LAS u32x4*)(L + L_XN + w * XN_STRIDE + lane * 16) = layernorm8(hv);
    wg_barrier();
    f32x4 ga = {0.f, 0.f, 0.f, 0.f}, ua = {0.f, 0.f, 0.f, 0.f};
#pragma unroll
    for (int t = 0; t < 2; ++t) {
      const u32x4 bx = *(LAS u32x4*)(L + L_XN + (c16 & 7) * XN_STRIDE + (64 * w + 32 * t + 8 * q4) * 2);
      ga = mfma32(wgf[t], bx, ga); ua = mfma32(wuf[t], bx, ua);
    }
    *(LAS f32x4*)(L + L_WQ + w * 2048 + lane * 16) = ga;
    *(LAS f32x4*)(L + L_WQ + w * 2048 + 1024 + lane * 16) = ua;
    wg_barrier();
    f32x4 gs = {0.f, 0.f, 0.f, 0.f}, us = {0.f, 0.f, 0.f, 0.f};
#pragma unroll
    for (int ww = 0; ww < 8; ++ww) { gs += *(LAS f32x4*)(L + L_WQ + ww * 2048 + lane * 16); us += *(LAS f32x4*)(L + L_WQ + ww * 2048 + 1024 + lane * 16); }
    float act[4];
#pragma unroll
    for (int j = 0; j < 4; ++j) {
      const unsigned gu = cvtpk(gs[j], us[j]);
      const float gv = blo(gu), uv = bhi(gu);
      const float z = 0.7978845608028654f * (gv + 0.044715f * gv * gv * gv);
      const float th = 1.0f - 2.0f * __builtin_amdgcn_rcpf(__builtin_amdgcn_exp2f(z * (2.0f * LOG2E)) + 1.0f);
      act[j] = bfr(0.5f * gv * (1.0f + th)) * uv;
    }
    const u32x2 ab2 = {cvtpk(act[0], act[1]), cvtpk(act[2], act[3])};
#pragma unroll
    for (int j = 0; j < 4; ++j) {
      const f32x4 d = mfma16(wdf[j], ab2, f32x4{0.f, 0.f, 0.f, 0.f});
      if (c16 < 8) {
        const int cg = w * 16 + j * 4 + q4, r = (c16 >> 2) * 128 + cg;
        const int dof = WS_D + (r * NFFN + pf) * 128 + (c16 & 3) * 32;
        __builtin_amdgcn_raw_buffer_store_b128(u32x4{asu(d[0]), tag, asu(d[1]), tag}, rws, dof, 0, AUX_SC1);
        __builtin_amdgcn_raw_buffer_store_b128(u32x4{asu(d[2]), tag, asu(d[3]), tag}, rws, dof + 16, 0, AUX_SC1);
      }
    }
  }
  {
    const int dof = WS_D + bid * (NFFN * 128) + tid * 16;
    const bool two = tid < (NFFN - 64) * 8;
    u32x4 g, g2 = {0u, tag, 0u, tag};
    for (unsigned spins = 0;;) {
      g = __builtin_amdgcn_raw_buffer_load_b128(rws, dof, 0, AUX_SC1);
      if (two) g2 = __builtin_amdgcn_raw_buffer_load_b128(rws, dof + 8192, 0, AUX_SC1);
      if (failed || all_lanes(g.y == tag && g.w == tag && g2.y == tag && g2.w == tag)) break;
      SPIN_STEP();
    }
    float a = asf(g.x) + asf(g2.x), b = asf(g.z) + asf(g2.z);
    a += dpp<ROR8>(a); b += dpp<ROR8>(b);
    a = xrow_sum(a); b = xrow_sum(b);
    if (lane < 8) *(LAS f32x2*)(L + L_RED + 1024 + (w * 8 + lane) * 8) = f32x2{a, b};
    wg_barrier();
    if (tid < 8) {
      float s0 = 0.f, s1 = 0.f;
#pragma unroll
      for (int ww = 0; ww < 8; ++ww) { const f32x2 v = *(LAS f32x2*)(L + L_RED + 1024 + (ww * 8 + tid) * 8); s0 += v.x; s1 += v.y; }
      const unsigned f = cvtpk(s0, s1);
      *(GAS unsigned*)(p.out + (rb * DM + rc) * 2) = cvtpk(blo(hres) + blo(f), bhi(hres) + bhi(f));
    }
    if (bid == 0 && tid == 0) __hip_atomic_store((GAS unsigned*)(p.ws + WS_GEN), tag, __ATOMIC_RELAXED, __HIP_MEMORY_SCOPE_AGENT);
  }
}

}

extern "C" void kernel_launch(void* const* d_in, const int* in_sizes, int n_in, void* d_out, int out_size, void* d_ws, size_t ws_size,
                              hipStream_t stream) {
  Params p;
  p.x = (const unsigned char*)d_in[0];
  p.k = (const unsigned char*)d_in[1];
  p.v = (const unsigned char*)d_in[2];
  p.wq = (const unsigned char*)d_in[3];
  p.wo = (const unsigned char*)d_in[4];
  p.wg = (const unsigned char*)d_in[6];
  p.wu = (const unsigned char*)d_in[7];
  p.wd = (const unsigned char*)d_in[8];
  p.out = (unsigned char*)d_out;
  p.ws = (unsigned char*)d_ws;
  fused_decode<<<NWG, NTHR, 0, stream>>>(p);
}
```

```cpp
#include <hip/hip_runtime.h>
#include <hip/hip_bf16.h>

namespace {

#define LAS __attribute__((address_space(3)))
#define GAS __attribute__((address_space(1)))
typedef unsigned u32x4 __attribute__((ext_vector_type(4)));
typedef unsigned u32x2 __attribute__((ext_vector_type(2)));
typedef float f32x4 __attribute__((ext_vector_type(4)));
typedef float f32x2 __attribute__((ext_vector_type(2)));
typedef short s16x4 __attribute__((ext_vector_type(4)));
typedef __bf16 bf16x8 __attribute__((ext_vector_type(8)));
typedef __bf16 bf16x2 __attribute__((ext_vector_type(2)));
typedef __amdgpu_buffer_rsrc_t rsrc_t;

constexpr int NB = 8, DM = 512, HQ = 32, HKV = 8, SK = 8192, DH = 128, FF = 1792;
constexpr int NWG = 256, NTHR = 512;
constexpr int NFFN = FF / 16;
constexpr float SCALE = 0.08838834764831845f, LOG2E = 1.4426950408889634f, EPS = 1e-5f;

#define RING_W 16
#define RING_W0 6
#define RING_W1 2
#define RING_D 12
#define LOCKSTEP 8
#define TQ 290
#define TK 80
#define TQE 5
#define DLYW 3
#define WEIGHTS_LATE 0
#define DLY1 12
#define DLY2 12
#define DLY3 12
#define DLY4 12
#define DLY4X 16
#define DLY4P 70
#define DLY4N 60
constexpr int W = RING_W, W0 = RING_W0, W1 = RING_W1, DEPTH = RING_D;
static_assert(W0 + W1 <= W && DEPTH <= W && DEPTH >= 2, "ring");
constexpr unsigned SPIN_CAP = 4000;

constexpr int WS_GEN = 0;
constexpr int WS_Q = 4096;
constexpr int WS_O = WS_Q + NB * HKV * 4 * 512 * 8;
constexpr int WS_ML = WS_O + 32 * 8 * 4 * 128 * 8;
constexpr int WS_Y = WS_ML + 32 * 8 * 4 * 16;
constexpr int WS_H = WS_Y + 128 * 32 * 256;
constexpr int WS_D = WS_H + 8 * 256 * 8;
constexpr int WS_END = WS_D + 256 * NFFN * 128;

constexpr int L_WQ = 0;
constexpr int L_WO = 16384;
constexpr int L_WG = 16384;
constexpr int L_WU = 32768;
constexpr int L_WD = 49152;
constexpr int XN_STRIDE = 1040;
constexpr int L_XN = 65536;
constexpr int L_RED = L_XN + 8 * XN_STRIDE;
constexpr int L_ML = L_RED + 8192;
constexpr int OB_STRIDE = 272;
constexpr int L_OB = L_ML + 256;
constexpr int L_QF = L_OB + 8 * OB_STRIDE;
constexpr int L_USED = L_QF + 1024;
constexpr int L_END = 86016;
static_assert(L_USED <= L_END, "LDS map");
static_assert(L_END <= 163840 && L_END > 81920, "one workgroup per CU");

struct Params {
  const unsigned char *x, *k, *v, *wq, *wo, *wg, *wu, *wd;
  unsigned char *out, *ws;
};

constexpr int AUX_NT = 2, AUX_SC1 = 16;
constexpr int RS3 = 0x00020000;

__device__ __forceinline__ rsrc_t mkrs(const void* p, int bytes) { return __builtin_amdgcn_make_buffer_rsrc((void*)p, 0, bytes, RS3); }
__device__ __forceinline__ float asf(unsigned u) { return __builtin_bit_cast(float, u); }
__device__ __forceinline__ unsigned asu(float f) { return __builtin_bit_cast(unsigned, f); }
__device__ __forceinline__ unsigned cvtpk(float lo, float hi) { f32x2 f = {lo, hi}; return __builtin_bit_cast(unsigned, __builtin_convertvector(f, bf16x2)); }
__device__ __forceinline__ float blo(unsigned u) { return asf(u << 16); }
__device__ __forceinline__ float bhi(unsigned u) { return asf(u & 0xffff0000u); }
__device__ __forceinline__ float bfr(float f) { return blo(cvtpk(f, f)); }
template <int C> __device__ __forceinline__ float dpp(float x) { return asf(__builtin_amdgcn_mov_dpp(asu(x), C, 0xf, 0xf, true)); }
template <int C> __device__ __forceinline__ unsigned dppu(unsigned x) { return __builtin_amdgcn_mov_dpp(x, C, 0xf, 0xf, true); }
constexpr int XOR1 = 0xB1, XOR2 = 0x4E, HMIRR = 0x141, MIRR = 0x140, ROR4 = 0x124, ROR8 = 0x128, ROR12 = 0x12C;
__device__ __forceinline__ float xrow_sum(float x) {
  auto s = __builtin_amdgcn_permlane16_swap(asu(x), asu(x), false, false);
  x = asf(s[0]) + asf(s[1]);
  auto t = __builtin_amdgcn_permlane32_swap(asu(x), asu(x), false, false);
  return asf(t[0]) + asf(t[1]);
}
__device__ __forceinline__ float xrow_max(float x) {
  auto s = __builtin_amdgcn_permlane16_swap(asu(x), asu(x), false, false);
  x = fmaxf(asf(s[0]), asf(s[1]));
  auto t = __builtin_amdgcn_permlane32_swap(asu(x), asu(x), false, false);
  return fmaxf(asf(t[0]), asf(t[1]));
}
__device__ __forceinline__ float wave_sum(float x) {
  x += dpp<XOR1>(x); x += dpp<XOR2>(x); x += dpp<HMIRR>(x); x += dpp<MIRR>(x);
  return xrow_sum(x);
}
__device__ __forceinline__ f32x4 mfma32(u32x4 a, u32x4 b, f32x4 c) {
  return __builtin_amdgcn_mfma_f32_16x16x32_bf16(__builtin_bit_cast(bf16x8, a), __builtin_bit_cast(bf16x8, b), c, 0, 0, 0);
}
__device__ __forceinline__ f32x4 mfma16(u32x2 a, u32x2 b, f32x4 c) {
  return __builtin_amdgcn_mfma_f32_16x16x16bf16_1k(__builtin_bit_cast(s16x4, a), __builtin_bit_cast(s16x4, b), c, 0, 0, 0);
}
__device__ __forceinline__ u32x2 trread(LAS unsigned char* p) { return __builtin_bit_cast(u32x2, __builtin_amdgcn_ds_read_tr16_b64_v4i16((LAS s16x4*)p)); }
__device__ __forceinline__ void wg_barrier() {
  asm volatile("s_waitcnt lgkmcnt(0)" ::: "memory");
  __builtin_amdgcn_s_barrier();
  asm volatile("" ::: "memory");
}
__device__ __forceinline__ void dma16(rsrc_t rs, unsigned voff, unsigned soff, unsigned ldsaddr) {
  unsigned keep;
  asm volatile("s_nop 4\n\ts_mov_b32 %0, m0\n\ts_mov_b32 m0, %1\n\ts_nop 0\n\tbuffer_load_dwordx4 %2, %3, %4 offen nt lds\n\ts_mov_b32 m0, %0"
               : "=&s"(keep) : "s"(ldsaddr), "v"(voff), "s"(rs), "s"(soff) : "memory");
}
__device__ __forceinline__ bool all_lanes(bool ok) { return __builtin_amdgcn_ballot_w64(!ok) == 0ull; }
#define SPIN_STEP() { asm volatile("" ::: "memory"); __builtin_amdgcn_s_sleep(1); if (++spins > SPIN_CAP) { failed = true; break; } }

__device__ __forceinline__ void pace(unsigned t0, unsigned ticks) {
  while ((unsigned)__builtin_amdgcn_s_memrealtime() - t0 < ticks) __builtin_amdgcn_s_sleep(1);
}
__device__ __forceinline__ u32x4 line_piece(f32x4 d, unsigned tag) {
  const unsigned a = __builtin_amdgcn_update_dpp(asu(d[0]), asu(d[2]), ROR8, 0xf, 0xc, false);
  const unsigned b = __builtin_amdgcn_update_dpp(asu(d[1]), asu(d[3]), ROR8, 0xf, 0xc, false);
  return u32x4{a, tag, b, tag};
}
__device__ __forceinline__ u32x4 layernorm8(const float (&v)[8]) {
  float s = ((v[0] + v[1]) + (v[2] + v[3])) + ((v[4] + v[5]) + (v[6] + v[7]));
  const float mean = wave_sum(s) * (1.0f / DM);
  float q = 0.f;
#pragma unroll
  for (int j = 0; j < 8; ++j) { const float d = v[j] - mean; q = fmaf(d, d, q); }
  const float var = bfr(wave_sum(q) * (1.0f / DM));
  const float mb = bfr(mean);
  const float den = bfr(__builtin_amdgcn_sqrtf(bfr(var + EPS)));
  const float r = __builtin_amdgcn_rcpf(den);
  u32x4 o;
#pragma unroll
  for (int j = 0; j < 4; ++j) {
    const unsigned d = cvtpk(v[2 * j] - mb, v[2 * j + 1] - mb);
    o[j] = cvtpk(blo(d) * r, bhi(d) * r);
  }
  return o;
}

__global__ void __launch_bounds__(NTHR, 2) fused_decode(Params p) {
  __shared__ __attribute__((aligned(16))) unsigned char smem[L_END];
  LAS unsigned char* L = (LAS unsigned char*)smem;
  const unsigned lbase = (unsigned)(size_t)L;
  const int tid = threadIdx.x, lane = tid & 63, w = __builtin_amdgcn_readfirstlane(tid >> 6);
  const int bid = blockIdx.x, xl = bid & 7, yl = bid >> 3;
  const int q4 = lane >> 4, c16 = lane & 15;
  const rsrc_t rws = mkrs(p.ws, WS_END);
  bool failed = false;

  const unsigned t_start = (unsigned)__builtin_amdgcn_s_memrealtime();
  const unsigned gen = __hip_atomic_load((GAS unsigned*)(p.ws + WS_GEN), __ATOMIC_RELAXED, __HIP_MEMORY_SCOPE_AGENT);
  const u32x4 xrow = *(const GAS u32x4*)(p.x + w * (DM * 2) + lane * 16);
  {
    const rsrc_t rq = mkrs(p.wq, DM * HQ * DH * 2);
    const unsigned vo = (unsigned)((128 * (yl & 3) + 16 * w + (lane >> 3)) * (HQ * DH * 2) + (512 * xl + 64 * (yl >> 2)) * 2 + (lane & 7) * 16);
    dma16(rq, vo, 0u, lbase + L_WQ + w * 2048);
    dma16(rq, vo, 8u * (HQ * DH * 2), lbase + L_WQ + w * 2048 + 1024);
  }
  const int ab = yl >> 2, ag = xl, as = yl & 3;
  const rsrc_t rk = mkrs(p.k + ((size_t)(ab * HKV + ag) * SK + as * 2048) * (DH * 2), 2048 * DH * 2);
  const rsrc_t rv = mkrs(p.v + ((size_t)(ab * HKV + ag) * SK + as * 2048) * (DH * 2), 2048 * DH * 2);
  const int vk = w * 4096 + ((lane >> 2) & 3) * 256 + (q4 * 4 + (lane & 3)) * 16;
  const int vv = w * 4096 + lane * 16;
  u32x4 ring[W];
#define LDN(n) ((n) < 64 ? __builtin_amdgcn_raw_buffer_load_b128(rk, vk + ((n) & 3) * 1024, ((n) >> 2) * 32768, AUX_NT) \
                         : __builtin_amdgcn_raw_buffer_load_b128(rv, vv + ((n) & 3) * 1024, (((n) - 64) >> 2) * 32768, AUX_NT))
  if (TK) pace(t_start, TK);
#pragma unroll
  for (int i = 0; i < W0; ++i) ring[i] = LDN(i);
  __builtin_amdgcn_sched_barrier(0);

  asm volatile("" :: "v"(gen));
  {
    float xv[8];
#pragma unroll
    for (int j = 0; j < 4; ++j) { xv[2 * j] = blo(xrow[j]); xv[2 * j + 1] = bhi(xrow[j]); }
    *(LAS u32x4*)(L + L_XN + w * XN_STRIDE + lane * 16) = layernorm8(xv);
  }
  asm volatile("s_waitcnt vmcnt(%0)" ::"i"(W0) : "memory");
  wg_barrier();
  const unsigned tag = __builtin_amdgcn_readfirstlane(gen) + 1u;
  if (w < 4) {
    f32x4 acc = {0.f, 0.f, 0.f, 0.f};
#pragma unroll
    for (int t = 0; t < 4; ++t) {
      LAS unsigned char* pa = L + L_WQ + (32 * t + 8 * q4 + (c16 >> 2)) * 128 + w * 32 + (c16 & 3) * 8;
      const u32x2 a0 = trread(pa), a1 = trread(pa + 4 * 128);
      const u32x4 bx = *(LAS u32x4*)(L + L_XN + (c16 & 7) * XN_STRIDE + (128 * (yl & 3) + 32 * t + 8 * q4) * 2);
      acc = mfma32(u32x4{a0.x, a0.y, a1.x, a1.y}, bx, acc);
    }
    const int col = 64 * (yl >> 2) + 16 * w + 4 * q4 + 2 * (c16 >> 3);
    __builtin_amdgcn_raw_buffer_store_b128(line_piece(acc, tag), rws, WS_Q + ((((c16 & 7) * 8 + xl) * 4 + (yl & 3)) * 512 + col) * 8, 0, AUX_SC1);
  }
  __builtin_amdgcn_sched_barrier(0);
  const bool proj = yl < 16, ffn = yl >= 16 && yl < 30;
  const int hd = 4 * xl + ((yl >> 2) & 3), nt = yl & 3;
  const int pf = 14 * xl + (yl - 16);
  auto proj_weights = [&]() {
    if (proj) {
      const rsrc_t ro = mkrs(p.wo, HQ * DH * DM * 2);
      const unsigned vo = (unsigned)((hd * 128 + 16 * w + q4) * (DM * 2) + nt * 256 + c16 * 16);
#pragma unroll
      for (int j = 0; j < 4; ++j) dma16(ro, vo, 4u * j * (DM * 2), lbase + L_WO + w * 4096 + j * 1024);
    }
  };
  if (w >= 4) {
    pace(t_start, TQ);
    const int qo = WS_Q + ((ab * 8 + ag) * 4 * 512 + 2 * (tid - 256)) * 8;
    u32x4 g[4];
#pragma unroll
    for (int k = 0; k < 4; ++k) g[k] = __builtin_amdgcn_raw_buffer_load_b128(rws, qo + k * 4096, 0, AUX_SC1);
#pragma unroll
    for (int i = W0; i < W; ++i) ring[i] = LDN(i);
    __builtin_amdgcn_sched_barrier(0);
    bool ok = true;
#pragma unroll
    for (int k = 0; k < 4; ++k) ok = ok && g[k].y == tag && g[k].w == tag;
    if (!all_lanes(ok)) {
      for (unsigned spins = 0;;) {
        SPIN_STEP();
        ok = true;
#pragma unroll
        for (int k = 0; k < 4; ++k) g[k] = __builtin_amdgcn_raw_buffer_load_b128(rws, qo + k * 4096, 0, AUX_SC1);
#pragma unroll
        for (int k = 0; k < 4; ++k) ok = ok && g[k].y == tag && g[k].w == tag;
        if (all_lanes(ok)) break;
      }
    }
    *(LAS unsigned*)(L + L_QF + (tid - 256) * 4) =
        cvtpk((asf(g[0].x) + asf(g[1].x)) + (asf(g[2].x) + asf(g[3].x)), (asf(g[0].z) + asf(g[1].z)) + (asf(g[2].z) + asf(g[3].z)));
  } else {
#pragma unroll
    for (int i = W0; i < W0 + W1; ++i) ring[i] = LDN(i);
    __builtin_amdgcn_sched_barrier(0);
    pace(t_start, TQ + TQE);
#pragma unroll
    for (int i = W0 + W1; i < W; ++i) ring[i] = LDN(i);
  }
  __builtin_amdgcn_sched_barrier(0);
  wg_barrier();
  proj_weights();
  const u32x4 qf = *(LAS u32x4*)(L + L_QF + (lane & 3) * 256 + q4 * 64 + ((lane >> 2) & 3) * 16);

  float T[16];
#pragma unroll
  for (int i = 0; i < 64; ++i) {
    const f32x4 d = mfma32(ring[i % W], qf, f32x4{0.f, 0.f, 0.f, 0.f});
    if (i + DEPTH >= W) ring[(i + DEPTH) % W] = LDN(i + DEPTH);
    const int x = (i & 7) >> 1, r = 2 * (i >> 3) + (i & 1);
    const float s = ((d[x] + dpp<ROR12>(d[(x + 1) & 3])) + dpp<ROR8>(d[(x + 2) & 3])) + dpp<ROR4>(d[(x + 3) & 3]);
    if (x == 0) T[r] = s;
    else if (x == 1) T[r] = asf(__builtin_amdgcn_update_dpp(asu(T[r]), asu(s), 0xE4, 0xf, 0x2, false));
    else if (x == 2) T[r] = asf(__builtin_amdgcn_update_dpp(asu(T[r]), asu(s), 0xE4, 0xf, 0x4, false));
    else T[r] = asf(__builtin_amdgcn_update_dpp(asu(T[r]), asu(s), 0xE4, 0xf, 0x8, false));
    if (LOCKSTEP && (i % LOCKSTEP) == LOCKSTEP - 1) __builtin_amdgcn_s_barrier();
    __builtin_amdgcn_sched_barrier(0);
  }
  unsigned P[8];
  float Mwg;
  {
    float mx = -INFINITY;
#pragma unroll
    for (int t = 0; t < 8; ++t) {
      const unsigned u = cvtpk(T[2 * t], T[2 * t + 1]);
      const unsigned u2 = cvtpk(blo(u) * SCALE, bhi(u) * SCALE);
      T[2 * t] = blo(u2); T[2 * t + 1] = bhi(u2);
      mx = fmaxf(mx, fmaxf(T[2 * t], T[2 * t + 1]));
    }
    mx = fmaxf(mx, dpp<ROR4>(mx)); mx = fmaxf(mx, dpp<ROR8>(mx)); mx = xrow_max(mx);
    if (lane < 4) *(LAS float*)(L + L_ML + (lane * 8 + w) * 4) = mx;
    wg_barrier();
    const f32x4 m0 = *(LAS f32x4*)(L + L_ML + (lane & 3) * 32), m1 = *(LAS f32x4*)(L + L_ML + (lane & 3) * 32 + 16);
    mx = fmaxf(fmaxf(fmaxf(m0[0], m0[1]), fmaxf(m0[2], m0[3])), fmaxf(fmaxf(m1[0], m1[1]), fmaxf(m1[2], m1[3])));
    float ls = 0.f;
#pragma unroll
    for (int t = 0; t < 8; ++t) {
      const float a = __builtin_amdgcn_exp2f((T[2 * t] - mx) * LOG2E), b = __builtin_amdgcn_exp2f((T[2 * t + 1] - mx) * LOG2E);
      ls += a + b;
      P[t] = cvtpk(a, b);
    }
    ls += dpp<ROR4>(ls); ls += dpp<ROR8>(ls);
    ls = xrow_sum(ls);
    if (lane < 4) *(LAS float*)(L + L_ML + 128 + (lane * 8 + w) * 4) = ls;
    Mwg = mx;
  }
  f32x4 acc[8];
#pragma unroll
  for (int j = 0; j < 8; ++j) acc[j] = f32x4{0.f, 0.f, 0.f, 0.f};
#pragma unroll
  for (int t = 0; t < 8; ++t) {
    u32x4 bf[8];
#pragma unroll
    for (int dw = 0; dw < 4; ++dw) {
      const int n0 = 64 + 8 * t + 2 * dw, n1 = n0 + 1;
      const u32x4 a = ring[n0 % W], b = ring[n1 % W];
#pragma unroll
      for (int e = 0; e < 4; ++e) {
        bf[2 * e][dw] = __builtin_amdgcn_perm(b[e], a[e], 0x05040100u);
        bf[2 * e + 1][dw] = __builtin_amdgcn_perm(b[e], a[e], 0x07060302u);
      }
      if (n0 + DEPTH < 128) ring[(n0 + DEPTH) % W] = LDN(n0 + DEPTH);
      if (n1 + DEPTH < 128) ring[(n1 + DEPTH) % W] = LDN(n1 + DEPTH);
      __builtin_amdgcn_sched_barrier(0);
    }
    const u32x4 af = {P[t], dppu<ROR12>(P[t]), dppu<ROR8>(P[t]), dppu<ROR4>(P[t])};
#pragma unroll
    for (int j = 0; j < 8; ++j) acc[j] = mfma32(af, bf[j], acc[j]);
    if (t == 0) {
      wg_barrier();
      if (w == 0 && lane < 4) {
        const f32x4 l0 = *(LAS f32x4*)(L + L_ML + 128 + lane * 32), l1 = *(LAS f32x4*)(L + L_ML + 128 + lane * 32 + 16);
        const float Ls = ((l0[0] + l0[1]) + (l0[2] + l0[3])) + ((l1[0] + l1[1]) + (l1[2] + l1[3]));
        __builtin_amdgcn_raw_buffer_store_b128(u32x4{asu(Mwg), tag, asu(Ls), tag}, rws, WS_ML + (((ag * 4 + lane) * 8 + ab) * 4 + as) * 16, 0, AUX_SC1);
      }
    } else if (LOCKSTEP && t < 7 && ((8 * t + 7) % LOCKSTEP) == LOCKSTEP - 1) __builtin_amdgcn_s_barrier();
    __builtin_amdgcn_sched_barrier(0);
  }
  if (lane < 16) {
#pragma unroll
    for (int j = 0; j < 8; ++j) *(LAS f32x4*)(L + L_WQ + w * 2048 + j * 256 + lane * 16) = acc[j];
  }
  asm volatile("s_waitcnt vmcnt(0)" ::: "memory");
  wg_barrier();
  if (w < 4) {
    float o0 = 0.f, o1 = 0.f;
#pragma unroll
    for (int ww = 0; ww < 8; ++ww) {
      LAS float* src = (LAS float*)(L + L_WQ + ww * 2048 + w * 512 + c16 * 16 + q4 * 4);
      o0 += src[0]; o1 += src[64];
    }
    __builtin_amdgcn_raw_buffer_store_b128(u32x4{asu(o0), tag, asu(o1), tag}, rws, WS_O + ((ag * 8 + ab) * 4 + as) * 4096 + w * 1024 + lane * 16, 0, AUX_SC1);
  }
  if (proj) {
    u32x4 wof[4];
#pragma unroll
    for (int t = 0; t < 4; ++t) {
      LAS unsigned char* pa = L + L_WO + (32 * t + 8 * q4 + (c16 >> 2)) * 256 + w * 32 + (c16 & 3) * 8;
      const u32x2 a0 = trread(pa), a1 = trread(pa + 4 * 256);
      wof[t] = u32x4{a0.x, a0.y, a1.x, a1.y};
    }
    const int rr = xl * 16 + yl;
    const unsigned xres = *(const GAS unsigned*)(p.x + (xl * DM + 32 * yl + 2 * (tid & 15)) * 2);
    {
      const int oo = WS_O + ((xl * 8 + w) * 4) * 4096 + q4 * 1024 + (hd & 3) * 256 + c16 * 16, mo = WS_ML + ((hd * 8 + w) * 4) * 16;
      float wt[4];
      {
        u32x4 ml[4];
        for (unsigned spins = 0;;) {
          bool ok = true;
#pragma unroll
          for (int s = 0; s < 4; ++s) ml[s] = __builtin_amdgcn_raw_buffer_load_b128(rws, mo + s * 16, 0, AUX_SC1);
#pragma unroll
          for (int s = 0; s < 4; ++s) ok = ok && ml[s].y == tag && ml[s].w == tag;
          if (all_lanes(ok)) break;
          SPIN_STEP();
        }
        const float M = fmaxf(fmaxf(asf(ml[0].x), asf(ml[1].x)), fmaxf(asf(ml[2].x), asf(ml[3].x)));
        float Ls = 0.f;
#pragma unroll
        for (int s = 0; s < 4; ++s) { wt[s] = __builtin_amdgcn_exp2f((asf(ml[s].x) - M) * LOG2E); Ls = fmaf(wt[s], asf(ml[s].z), Ls); }
        const float rl = __builtin_amdgcn_rcpf(Ls);
#pragma unroll
        for (int s = 0; s < 4; ++s) wt[s] *= rl;
        asm volatile("" : "+v"(wt[0]), "+v"(wt[1]), "+v"(wt[2]), "+v"(wt[3]));
      }
      u32x4 og[4];
      if (DLY1) __builtin_amdgcn_s_sleep(DLY1);
      for (unsigned spins = 0;;) {
        bool ok = true;
#pragma unroll
        for (int s = 0; s < 4; ++s) og[s] = __builtin_amdgcn_raw_buffer_load_b128(rws, oo + s * 4096, 0, AUX_SC1);
#pragma unroll
        for (int s = 0; s < 4; ++s) ok = ok && og[s].y == tag && og[s].w == tag;
        if (failed || all_lanes(ok)) break;
        SPIN_STEP();
      }
      float O0 = 0.f, O1 = 0.f;
#pragma unroll
      for (int s = 0; s < 4; ++s) { O0 = fmaf(wt[s], asf(og[s].x), O0); O1 = fmaf(wt[s], asf(og[s].z), O1); }
      *(LAS unsigned*)(L + L_OB + w * OB_STRIDE + c16 * 16 + q4 * 4) = cvtpk(O0, O1);
      wg_barrier();
      f32x4 y = {0.f, 0.f, 0.f, 0.f};
#pragma unroll
      for (int t = 0; t < 4; ++t) y = mfma32(wof[t], *(LAS u32x4*)(L + L_OB + (c16 & 7) * OB_STRIDE + (32 * t + 8 * q4) * 2), y);
      const int r = (c16 & 7) * 16 + nt * 4 + (w >> 1);
      __builtin_amdgcn_raw_buffer_store_b128(line_piece(y, tag), rws, WS_Y + (r * 32 + hd) * 256 + (w & 1) * 128 + q4 * 32 + (c16 >> 3) * 16, 0, AUX_SC1);
    }
    {
      const int yo = WS_Y + rr * 8192 + tid * 16;
      u32x4 g;
      if (DLY2) __builtin_amdgcn_s_sleep(DLY2);
      for (unsigned spins = 0;;) {
        g = __builtin_amdgcn_raw_buffer_load_b128(rws, yo, 0, AUX_SC1);
        if (failed || all_lanes(g.y == tag && g.w == tag)) break;
        SPIN_STEP();
      }
      const float a = xrow_sum(asf(g.x)), b = xrow_sum(asf(g.z));
      if (lane < 16) *(LAS f32x2*)(L + L_RED + (w * 16 + lane) * 8) = f32x2{a, b};
      wg_barrier();
      if (tid < 16) {
        float s0 = 0.f, s1 = 0.f;
#pragma unroll
        for (int ww = 0; ww < 8; ++ww) { const f32x2 v = *(LAS f32x2*)(L + L_RED + (ww * 16 + tid) * 8); s0 += v.x; s1 += v.y; }
        const unsigned at = cvtpk(s0, s1);
        const unsigned hv = cvtpk(blo(xres) + blo(at), bhi(xres) + bhi(at));
        __builtin_amdgcn_raw_buffer_store_b64(u32x2{hv, tag}, rws, WS_H + (xl * 256 + 16 * yl + tid) * 8, 0, AUX_SC1);
      }
    }
  } else if (ffn) {
    {
      const rsrc_t rg = mkrs(p.wg, DM * FF * 2), ru = mkrs(p.wu, DM * FF * 2), rd = mkrs(p.wd, FF * DM * 2);
      const unsigned vg = (unsigned)((64 * w + (lane >> 1)) * (FF * 2) + pf * 32 + (lane & 1) * 16);
      dma16(rg, vg, 0u, lbase + L_WG + w * 2048);
      dma16(rg, vg, 32u * (FF * 2), lbase + L_WG + w * 2048 + 1024);
      dma16(ru, vg, 0u, lbase + L_WU + w * 2048);
      dma16(ru, vg, 32u * (FF * 2), lbase + L_WU + w * 2048 + 1024);
      const unsigned vd = (unsigned)((pf * 16 + 2 * w) * (DM * 2) + lane * 16);
      dma16(rd, vd, 0u, lbase + L_WD + w * 2048);
      dma16(rd, vd, (unsigned)(DM * 2), lbase + L_WD + w * 2048 + 1024);
    }
    asm volatile("s_waitcnt vmcnt(0)" ::: "memory");
    wg_barrier();
    u32x4 wgf[2], wuf[2];
    u32x2 wdf[8];
#pragma unroll
    for (int t = 0; t < 2; ++t) {
      LAS unsigned char* pg = L + L_WG + (64 * w + 32 * t + 8 * q4 + (c16 >> 2)) * 32 + (c16 & 3) * 8;
      const u32x2 g0 = trread(pg), g1 = trread(pg + 128), u0 = trread(pg + (L_WU - L_WG)), u1 = trread(pg + (L_WU - L_WG) + 128);
      wgf[t] = u32x4{g0.x, g0.y, g1.x, g1.y};
      wuf[t] = u32x4{u0.x, u0.y, u1.x, u1.y};
    }
#pragma unroll
    for (int j = 0; j < 8; ++j) wdf[j] = trread(L + L_WD + (4 * q4 + (c16 >> 2)) * 1024 + (128 * (w & 3) + 16 * j) * 2 + (c16 & 3) * 8);
    const u32x4 ones = {0x3f803f80u, 0x3f803f80u, 0x3f803f80u, 0x3f803f80u};
    f32x4 cs = {0.f, 0.f, 0.f, 0.f};
    {
      f32x4 cg = {0.f, 0.f, 0.f, 0.f}, cu = {0.f, 0.f, 0.f, 0.f};
#pragma unroll
      for (int t = 0; t < 2; ++t) { cg = mfma32(wgf[t], ones, cg); cu = mfma32(wuf[t], ones, cu); }
      *(LAS f32x4*)(L + L_WQ + w * 1024 + lane * 16) = c16 < 8 ? cg : cu;
      wg_barrier();
#pragma unroll
      for (int ww = 0; ww < 8; ++ww) cs += *(LAS f32x4*)(L + L_WQ + ww * 1024 + lane * 16);
      asm volatile("" : "+v"(cs));
#pragma unroll
      for (int j = 0; j < 8; ++j) asm volatile("" : "+v"(wdf[j]));
    }
    const int ho = WS_H + (c16 & 7) * 2048 + (32 * w + 4 * q4 + 2 * (c16 >> 3)) * 8;
    const bool diag = c16 < 8 && q4 == (c16 >> 2);
    u32x4 g0, g1;
    if (DLY3) __builtin_amdgcn_s_sleep(DLY3);
    for (unsigned spins = 0;;) {
      g0 = __builtin_amdgcn_raw_buffer_load_b128(rws, ho, 0, AUX_SC1); g1 = __builtin_amdgcn_raw_buffer_load_b128(rws, ho + 128, 0, AUX_SC1);
      if (all_lanes(g0.y == tag && g0.w == tag && g1.y == tag && g1.w == tag)) break;
      SPIN_STEP();
    }
    {
      const u32x4 b0 = {g0.x, g0.z, dppu<ROR8>(g0.x), dppu<ROR8>(g0.z)}, b1 = {g1.x, g1.z, dppu<ROR8>(g1.x), dppu<ROR8>(g1.z)};
      const f32x4 z4 = {0.f, 0.f, 0.f, 0.f};
      f32x4 ga = mfma32(wgf[0], b0, z4), ua = mfma32(wuf[0], b0, z4);
      f32x4 s1 = mfma32(ones, b0, z4), s2 = mfma32(b0, b0, z4);
      ga = mfma32(wgf[1], b1, ga); ua = mfma32(wuf[1], b1, ua);
      s1 = mfma32(ones, b1, s1); s2 = mfma32(b1, b1, s2);
      f32x4 gu;
#pragma unroll
      for (int j = 0; j < 4; ++j) gu[j] = asf(__builtin_amdgcn_update_dpp(asu(ga[j]), asu(ua[j]), ROR8, 0xf, 0xc, false));
      *(LAS f32x4*)(L + L_WQ + 8192 + w * 1024 + lane * 16) = gu;
      const float sq = (c16 & 2) ? ((c16 & 1) ? s2[3] : s2[2]) : ((c16 & 1) ? s2[1] : s2[0]);
      if (diag) *(LAS f32x2*)(L + L_RED + (c16 * 8 + w) * 8) = f32x2{s1[0], sq};
    }
    wg_barrier();
    if (w < 4) {
      f32x4 X = {0.f, 0.f, 0.f, 0.f};
#pragma unroll
      for (int ww = 0; ww < 8; ++ww) X += *(LAS f32x4*)(L + L_WQ + 8192 + ww * 1024 + lane * 16);
      f32x4 sv = {0.f, 0.f, 0.f, 0.f};
#pragma unroll
      for (int ww = 0; ww < 4; ++ww) sv += *(LAS f32x4*)(L + L_RED + (c16 & 7) * 64 + ww * 16);
      const float mean = (sv[0] + sv[2]) * (1.0f / DM);
      const float var = bfr(fmaxf((sv[1] + sv[3]) * (1.0f / DM) - mean * mean, 0.f));
      const float mb = bfr(mean);
      const float rd = __builtin_amdgcn_rcpf(bfr(__builtin_amdgcn_sqrtf(bfr(var + EPS))));
      const unsigned p01 = cvtpk((X[0] - mb * cs[0]) * rd, (X[1] - mb * cs[1]) * rd), p23 = cvtpk((X[2] - mb * cs[2]) * rd, (X[3] - mb * cs[3]) * rd);
      const unsigned gp = __builtin_amdgcn_update_dpp(p01, p23, ROR8, 0xf, 0xc, false), up = __builtin_amdgcn_update_dpp(p23, p01, ROR8, 0xf, 0x3, false);
      float act[2];
#pragma unroll
      for (int j = 0; j < 2; ++j) {
        const float gv = j ? bhi(gp) : blo(gp), uv = j ? bhi(up) : blo(up);
        const float e = __builtin_amdgcn_exp2f(gv * fmaf(gv * gv, -2.0f * LOG2E * 0.7978845608028654f * 0.044715f, -2.0f * LOG2E * 0.7978845608028654f));
        act[j] = bfr(gv * __builtin_amdgcn_rcpf(1.0f + e)) * uv;
      }
      const unsigned ap = cvtpk(act[0], act[1]);
      const u32x2 ab2 = {ap, dppu<ROR8>(ap)};
      f32x4 d[8];
#pragma unroll
      for (int j = 0; j < 8; ++j) d[j] = mfma16(wdf[j], ab2, f32x4{0.f, 0.f, 0.f, 0.f});
      __builtin_amdgcn_sched_barrier(0);
      const int dof = WS_D + (((c16 & 7) * 32 + w * 8) * NFFN + pf) * 128 + q4 * 32 + (c16 >> 3) * 16;
#pragma unroll
      for (int j = 0; j < 8; ++j) __builtin_amdgcn_raw_buffer_store_b128(line_piece(d[j], tag), rws, dof + j * (NFFN * 128), 0, AUX_SC1);
    }
  }
  {
    const int dof = WS_D + bid * (NFFN * 128) + tid * 16;
    const int hof = WS_H + ((bid >> 5) * 256 + 8 * (bid & 31) + (tid & 7)) * 8;
    const bool two = tid < (NFFN - 64) * 8;
    u32x4 g, g2 = {0u, tag, 0u, tag};
    u32x2 hg;
    if (proj) { if (DLY4P) __builtin_amdgcn_s_sleep(DLY4P); }
    else if (ffn) { if (w < 4) __builtin_amdgcn_s_sleep(DLY4); else __builtin_amdgcn_s_sleep(DLY4 + DLY4X); }
    else { if (DLY4N) __builtin_amdgcn_s_sleep(DLY4N); if (DLY4N) __builtin_amdgcn_s_sleep(DLY4N); }
    for (unsigned spins = 0;;) {
      g = __builtin_amdgcn_raw_buffer_load_b128(rws, dof, 0, AUX_SC1);
      if (two) g2 = __builtin_amdgcn_raw_buffer_load_b128(rws, dof + 8192, 0, AUX_SC1);
      hg = __builtin_amdgcn_raw_buffer_load_b64(rws, hof, 0, AUX_SC1);
      if (failed || all_lanes(g.y == tag && g.w == tag && g2.y == tag && g2.w == tag && hg.y == tag)) break;
      SPIN_STEP();
    }
    float a = asf(g.x) + asf(g2.x), b = asf(g.z) + asf(g2.z);
    a += dpp<ROR8>(a); b += dpp<ROR8>(b);
    a = xrow_sum(a); b = xrow_sum(b);
    if (lane < 8) *(LAS f32x2*)(L + L_RED + 1024 + (w * 8 + lane) * 8) = f32x2{a, b};
    wg_barrier();
    if (tid < 8) {
      float s0 = 0.f, s1 = 0.f;
#pragma unroll
      for (int ww = 0; ww < 8; ++ww) { const f32x2 v = *(LAS f32x2*)(L + L_RED + 1024 + (ww * 8 + tid) * 8); s0 += v.x; s1 += v.y; }
      const unsigned f = cvtpk(s0, s1);
      *(GAS unsigned*)(p.out + ((bid >> 5) * DM + 16 * (bid & 31) + 2 * tid) * 2) = cvtpk(blo(hg.x) + blo(f), bhi(hg.x) + bhi(f));
    }
    if (bid == 0 && tid == 0) __hip_atomic_store((GAS unsigned*)(p.ws + WS_GEN), tag, __ATOMIC_RELAXED, __HIP_MEMORY_SCOPE_AGENT);
  }
}

}

extern "C" void kernel_launch(void* const* d_in, const int* in_sizes, int n_in, void* d_out, int out_size, void* d_ws, size_t ws_size,
                              hipStream_t stream) {
  Params p;
  p.x = (const unsigned char*)d_in[0];
  p.k = (const unsigned char*)d_in[1];
  p.v = (const unsigned char*)d_in[2];
  p.wq = (const unsigned char*)d_in[3];
  p.wo = (const unsigned char*)d_in[4];
  p.wg = (const unsigned char*)d_in[6];
  p.wu = (const unsigned char*)d_in[7];
  p.wd = (const unsigned char*)d_in[8];
  p.out = (unsigned char*)d_out;
  p.ws = (unsigned char*)d_ws;
  fused_decode<<<NWG, NTHR, 0, stream>>>(p);
}
```

```cpp
#include <hip/hip_runtime.h>
#include <hip/hip_bf16.h>

namespace {

#define LAS __attribute__((address_space(3)))
#define GAS __attribute__((address_space(1)))
typedef unsigned u32x4 __attribute__((ext_vector_type(4)));
typedef unsigned u32x2 __attribute__((ext_vector_type(2)));
typedef float f32x4 __attribute__((ext_vector_type(4)));
typedef float f32x2 __attribute__((ext_vector_type(2)));
typedef short s16x4 __attribute__((ext_vector_type(4)));
typedef __bf16 bf16x8 __attribute__((ext_vector_type(8)));
typedef __bf16 bf16x2 __attribute__((ext_vector_type(2)));
typedef __amdgpu_buffer_rsrc_t rsrc_t;

constexpr int NB = 8, DM = 512, HQ = 32, HKV = 8, SK = 8192, DH = 128, FF = 1792;
constexpr int NWG = 256, NTHR = 512;
constexpr int NFFN = FF / 16;
constexpr float SCALE = 0.08838834764831845f, LOG2E = 1.4426950408889634f, EPS = 1e-5f;

constexpr int RING_W = 16;
constexpr int RING_W0 = 5;
constexpr int RING_W1 = 4;
constexpr int RING_D = 12;
constexpr int TK = 50;
constexpr int TQ = 270;
constexpr int TQL = 65;
constexpr int T1 = 70;
constexpr int DLY2 = 12;
constexpr int DLY3 = 12;
constexpr int DLY4 = 65;
constexpr int W = RING_W, W0 = RING_W0, W1 = RING_W1, DEPTH = RING_D;
static_assert(W0 + W1 <= W && DEPTH <= W && DEPTH >= 2 && W < 62, "ring");
constexpr int MLT = 5;
constexpr unsigned SPIN_CAP = 50000;

constexpr int WS_GEN = 0;
constexpr int WS_Q = 4096;
constexpr int WS_O = WS_Q + NB * HKV * 4 * 512 * 8;
constexpr int WS_ML = WS_O + 32 * 8 * 4 * 128 * 8;
constexpr int WS_Y = WS_ML + 32 * 8 * 4 * 16;
constexpr int WS_H = WS_Y + 128 * 32 * 256;
constexpr int WS_D = WS_H + 8 * 256 * 8;
constexpr int WS_F = WS_D + 128 * NFFN * 128;
constexpr int WS_END = WS_F + 256 * 128;

constexpr int L_WQ = 0;
constexpr int L_WO = 16384;
constexpr int L_WG = 16384;
constexpr int L_WU = 32768;
constexpr int L_WD = 49152;
constexpr int XN_STRIDE = 1040;
constexpr int L_XN = 65536;
constexpr int L_RED = L_XN + 8 * XN_STRIDE;
constexpr int L_ML = L_RED + 8192;
constexpr int OB_STRIDE = 272;
constexpr int L_OB = L_ML + 256;
constexpr int L_QF = L_OB + 8 * OB_STRIDE;
constexpr int L_FLEX = L_QF + 1024;
constexpr int L_USED = L_FLEX + 16;
constexpr int L_END = 86016;
static_assert(L_USED <= L_END, "LDS map");
static_assert(L_END > 81920, "one workgroup per CU");

struct Params {
  const unsigned char *x, *k, *v, *wq, *wo, *wg, *wu, *wd;
  unsigned char *out, *ws;
};

constexpr int AUX_NT = 2, AUX_SC1 = 16;
constexpr int RS3 = 0x00020000;

__device__ __forceinline__ rsrc_t mkrs(const void* p, int bytes) { return __builtin_amdgcn_make_buffer_rsrc((void*)p, 0, bytes, RS3); }
__device__ __forceinline__ float asf(unsigned u) { return __builtin_bit_cast(float, u); }
__device__ __forceinline__ unsigned asu(float f) { return __builtin_bit_cast(unsigned, f); }
__device__ __forceinline__ unsigned cvtpk(float lo, float hi) { f32x2 f = {lo, hi}; return __builtin_bit_cast(unsigned, __builtin_convertvector(f, bf16x2)); }
__device__ __forceinline__ float blo(unsigned u) { return asf(u << 16); }
__device__ __forceinline__ float bhi(unsigned u) { return asf(u & 0xffff0000u); }
__device__ __forceinline__ float bfr(float f) { return blo(cvtpk(f, f)); }
template <int C> __device__ __forceinline__ float dpp(float x) { return asf(__builtin_amdgcn_mov_dpp(asu(x), C, 0xf, 0xf, true)); }
template <int C> __device__ __forceinline__ unsigned dppu(unsigned x) { return __builtin_amdgcn_mov_dpp(x, C, 0xf, 0xf, true); }
constexpr int XOR1 = 0xB1, XOR2 = 0x4E, HMIRR = 0x141, MIRR = 0x140, ROR4 = 0x124, ROR8 = 0x128, ROR12 = 0x12C;
__device__ __forceinline__ float xrow_sum(float x) {
  auto s = __builtin_amdgcn_permlane16_swap(asu(x), asu(x), false, false);
  x = asf(s[0]) + asf(s[1]);
  auto t = __builtin_amdgcn_permlane32_swap(asu(x), asu(x), false, false);
  return asf(t[0]) + asf(t[1]);
}
__device__ __forceinline__ float xrow_max(float x) {
  auto s = __builtin_amdgcn_permlane16_swap(asu(x), asu(x), false, false);
  x = fmaxf(asf(s[0]), asf(s[1]));
  auto t = __builtin_amdgcn_permlane32_swap(asu(x), asu(x), false, false);
  return fmaxf(asf(t[0]), asf(t[1]));
}
__device__ __forceinline__ float wave_sum(float x) {
  x += dpp<XOR1>(x); x += dpp<XOR2>(x); x += dpp<HMIRR>(x); x += dpp<MIRR>(x);
  return xrow_sum(x);
}
__device__ __forceinline__ f32x4 mfma32(u32x4 a, u32x4 b, f32x4 c) {
  return __builtin_amdgcn_mfma_f32_16x16x32_bf16(__builtin_bit_cast(bf16x8, a), __builtin_bit_cast(bf16x8, b), c, 0, 0, 0);
}
__device__ __forceinline__ f32x4 mfma16(u32x2 a, u32x2 b, f32x4 c) {
  return __builtin_amdgcn_mfma_f32_16x16x16bf16_1k(__builtin_bit_cast(s16x4, a), __builtin_bit_cast(s16x4, b), c, 0, 0, 0);
}
__device__ __forceinline__ u32x2 trread(LAS unsigned char* p) { return __builtin_bit_cast(u32x2, __builtin_amdgcn_ds_read_tr16_b64_v4i16((LAS s16x4*)p)); }
__device__ __forceinline__ void wg_barrier() {
  asm volatile("s_waitcnt lgkmcnt(0)" ::: "memory");
  __builtin_amdgcn_s_barrier();
  asm volatile("" ::: "memory");
}
__device__ __forceinline__ void dma16(rsrc_t rs, unsigned voff, unsigned soff, unsigned ldsaddr) {
  unsigned keep;
  asm volatile("s_nop 4\n\ts_mov_b32 %0, m0\n\ts_mov_b32 m0, %1\n\ts_nop 0\n\tbuffer_load_dwordx4 %2, %3, %4 offen nt lds\n\ts_mov_b32 m0, %0"
               : "=&s"(keep) : "s"(ldsaddr), "v"(voff), "s"(rs), "s"(soff) : "memory");
}
__device__ __forceinline__ bool all_lanes(bool ok) { return __builtin_amdgcn_ballot_w64(!ok) == 0ull; }
#define SPIN_STEP() { asm volatile("" ::: "memory"); __builtin_amdgcn_s_sleep(1); if (++spins > SPIN_CAP) { failed = true; break; } }

__device__ __forceinline__ void pace(unsigned t0, unsigned ticks) {
  while ((unsigned)__builtin_amdgcn_s_memrealtime() - t0 < ticks) __builtin_amdgcn_s_sleep(1);
}
__device__ __forceinline__ u32x4 line_piece(f32x4 d, unsigned tag) {
  const unsigned a = __builtin_amdgcn_update_dpp(asu(d[0]), asu(d[2]), ROR8, 0xf, 0xc, false);
  const unsigned b = __builtin_amdgcn_update_dpp(asu(d[1]), asu(d[3]), ROR8, 0xf, 0xc, false);
  return u32x4{a, tag, b, tag};
}
__device__ __forceinline__ u32x4 pack4(float a0, float a1, float a2, float a3, unsigned tA, unsigned tB) {
  const unsigned u0 = asu(a0) + 0x80u, u1 = asu(a1) + 0x80u, u2 = asu(a2) + 0x80u, u3 = asu(a3) + 0x80u;
  return u32x4{__builtin_amdgcn_perm(u0, u1, 0x07060503u), __builtin_amdgcn_perm(u1, tA, 0x06050100u),
               __builtin_amdgcn_perm(u2, u3, 0x07060503u), __builtin_amdgcn_perm(u3, tB, 0x06050100u)};
}
__device__ __forceinline__ f32x4 unpack4(u32x4 g) {
  return f32x4{asf(g.x & 0xffffff00u), asf(__builtin_amdgcn_perm(g.x, g.y, 0x0403020cu)), asf(g.z & 0xffffff00u), asf(__builtin_amdgcn_perm(g.z, g.w, 0x0403020cu))};
}
__device__ __forceinline__ bool tags4(u32x4 g, unsigned tA, unsigned tB) { return (g.y & 0xffffu) == tA && (g.w & 0xffffu) == tB; }
__device__ __forceinline__ u32x4 layernorm8(const float (&v)[8]) {
  float s = ((v[0] + v[1]) + (v[2] + v[3])) + ((v[4] + v[5]) + (v[6] + v[7]));
  const float mean = wave_sum(s) * (1.0f / DM);
  float q = 0.f;
#pragma unroll
  for (int j = 0; j < 8; ++j) { const float d = v[j] - mean; q = fmaf(d, d, q); }
  const float var = bfr(wave_sum(q) * (1.0f / DM));
  const float mb = bfr(mean);
  const float den = bfr(__builtin_amdgcn_sqrtf(bfr(var + EPS)));
  const float r = __builtin_amdgcn_rcpf(den);
  u32x4 o;
#pragma unroll
  for (int j = 0; j < 4; ++j) {
    const unsigned d = cvtpk(v[2 * j] - mb, v[2 * j + 1] - mb);
    o[j] = cvtpk(blo(d) * r, bhi(d) * r);
  }
  return o;
}

__global__ void __launch_bounds__(NTHR, 2) fused_decode(Params p) {
  __shared__ __attribute__((aligned(16))) unsigned char smem[L_END];
  LAS unsigned char* L = (LAS unsigned char*)smem;
  const unsigned lbase = (unsigned)(size_t)L;
  const int tid = threadIdx.x, lane = tid & 63, w = __builtin_amdgcn_readfirstlane(tid >> 6);
  const int bid = blockIdx.x, xl = bid & 7, yl = bid >> 3;
  const int q4 = lane >> 4, c16 = lane & 15;
  const rsrc_t rws = mkrs(p.ws, WS_END);
  bool failed = false;

  const unsigned t_start = (unsigned)__builtin_amdgcn_s_memrealtime();
  const unsigned gen = __hip_atomic_load((GAS unsigned*)(p.ws + WS_GEN), __ATOMIC_RELAXED, __HIP_MEMORY_SCOPE_AGENT);
  const u32x4 xrow = *(const GAS u32x4*)(p.x + w * (DM * 2) + lane * 16);
  {
    const rsrc_t rq = mkrs(p.wq, DM * HQ * DH * 2);
    const unsigned vo = (unsigned)((128 * (yl & 3) + 16 * w + (lane >> 3)) * (HQ * DH * 2) + (512 * xl + 64 * (yl >> 2)) * 2 + (lane & 7) * 16);
    dma16(rq, vo, 0u, lbase + L_WQ + w * 2048);
    dma16(rq, vo, 8u * (HQ * DH * 2), lbase + L_WQ + w * 2048 + 1024);
  }
  const int ab = yl >> 2, ag = (xl & 6) + ((yl >> 1) & 1), as = 2 * (yl & 1) + (xl & 1);
  const rsrc_t rk = mkrs(p.k + (size_t)(ab * HKV + ag) * SK * (DH * 2), SK * DH * 2);
  const rsrc_t rv = mkrs(p.v + (size_t)(ab * HKV + ag) * SK * (DH * 2), SK * DH * 2);
  const int vk = w * 4096 + ((lane >> 2) & 3) * 256 + (q4 * 4 + (lane & 3)) * 16;
  const int vv = w * 4096 + lane * 16;
  const int sown = as * 524288, spar = (as ^ 3) * 524288 + 15 * 32768;
  int vka = vk, vva = vv, vkb = vk, vvb = vv;
  u32x4 ring[W];
  constexpr int NK = 66;
#define LD1(rs, vo, voa, vob, n, aux) ((n) < 62 ? __builtin_amdgcn_raw_buffer_load_b128(rs, vo + ((n) & 3) * 1024, sown + ((n) >> 2) * 32768, aux) \
                                     : (n) < 64 ? __builtin_amdgcn_raw_buffer_load_b128(rs, voa + ((n) & 3) * 1024, sown + 15 * 32768, aux) \
                                                : __builtin_amdgcn_raw_buffer_load_b128(rs, vob + ((n) - 62) * 1024, spar, aux))
#define LDN(n) ((n) < NK ? LD1(rk, vk, vka, vkb, (n), AUX_NT) : LD1(rv, vv, vva, vvb, (n) - NK, AUX_NT))
  pace(t_start, TK);
#pragma unroll
  for (int i = 0; i < W0; ++i) ring[i] = LDN(i);
  __builtin_amdgcn_sched_barrier(0);

  {
    float xv[8];
#pragma unroll
    for (int j = 0; j < 4; ++j) { xv[2 * j] = blo(xrow[j]); xv[2 * j + 1] = bhi(xrow[j]); }
    *(LAS u32x4*)(L + L_XN + w * XN_STRIDE + lane * 16) = layernorm8(xv);
  }
  asm volatile("s_waitcnt vmcnt(%0)" ::"i"(W0) : "memory");
  wg_barrier();
  const unsigned t_land = (unsigned)__builtin_amdgcn_s_memrealtime() - t_start + TQL, t_look = t_land > TQ ? t_land : TQ;
  const unsigned tag = __builtin_amdgcn_readfirstlane(gen) + 1u;
  const unsigned tagA = tag & 0xffffu, tagB = (tag ^ (tag >> 16)) & 0xffffu;
  const unsigned slow = (unsigned)__builtin_amdgcn_s_getreg(20 | (3 << 11)) & 1u;
  if (w < 4) {
    f32x4 acc = {0.f, 0.f, 0.f, 0.f};
#pragma unroll
    for (int t = 0; t < 4; ++t) {
      LAS unsigned char* pa = L + L_WQ + (32 * t + 8 * q4 + (c16 >> 2)) * 128 + w * 32 + (c16 & 3) * 8;
      const u32x2 a0 = trread(pa), a1 = trread(pa + 4 * 128);
      const u32x4 bx = *(LAS u32x4*)(L + L_XN + (c16 & 7) * XN_STRIDE + (128 * (yl & 3) + 32 * t + 8 * q4) * 2);
      acc = mfma32(u32x4{a0.x, a0.y, a1.x, a1.y}, bx, acc);
    }
    if (tid == 0) __builtin_amdgcn_raw_buffer_store_b64(u32x2{slow, tag}, rws, WS_F + bid * 128, 0, AUX_SC1);
    const int col = 64 * (yl >> 2) + 16 * w + 4 * q4 + 2 * (c16 >> 3);
    __builtin_amdgcn_raw_buffer_store_b128(line_piece(acc, tag), rws, WS_Q + ((((c16 & 7) * 8 + xl) * 4 + (yl & 3)) * 512 + col) * 8, 0, AUX_SC1);
  }
  __builtin_amdgcn_sched_barrier(0);
  if (w >= 4) {
#pragma unroll
    for (int i = W0; i < W0 + W1; ++i) ring[i] = LDN(i);
    __builtin_amdgcn_sched_barrier(0);
    pace(t_start, t_look);
    const int qo = WS_Q + ((ab * 8 + ag) * 4 * 512 + 2 * (tid - 256)) * 8;
    const int fo = WS_F + (bid ^ 9) * 128;
    u32x4 g[4];
#pragma unroll
    for (int k = 0; k < 4; ++k) g[k] = __builtin_amdgcn_raw_buffer_load_b128(rws, qo + k * 4096, 0, AUX_SC1);
    u32x2 fg = __builtin_amdgcn_raw_buffer_load_b64(rws, fo, 0, AUX_SC1);
#pragma unroll
    for (int i = W0 + W1; i < W; ++i) ring[i] = LDN(i);
    __builtin_amdgcn_sched_barrier(0);
    bool ok = fg.y == tag;
#pragma unroll
    for (int k = 0; k < 4; ++k) ok = ok && g[k].y == tag && g[k].w == tag;
    if (!all_lanes(ok)) {
      for (unsigned spins = 0;;) {
        SPIN_STEP();
#pragma unroll
        for (int k = 0; k < 4; ++k) g[k] = __builtin_amdgcn_raw_buffer_load_b128(rws, qo + k * 4096, 0, AUX_SC1);
        fg = __builtin_amdgcn_raw_buffer_load_b64(rws, fo, 0, AUX_SC1);
        ok = fg.y == tag;
#pragma unroll
        for (int k = 0; k < 4; ++k) ok = ok && g[k].y == tag && g[k].w == tag;
        if (all_lanes(ok)) break;
      }
    }
    if (tid == 256) *(LAS unsigned*)(L + L_FLEX) = fg.x;
    *(LAS unsigned*)(L + L_QF + (tid - 256) * 4) =
        cvtpk((asf(g[0].x) + asf(g[1].x)) + (asf(g[2].x) + asf(g[3].x)), (asf(g[0].z) + asf(g[1].z)) + (asf(g[2].z) + asf(g[3].z)));
  } else {
#pragma unroll
    for (int i = W0; i < W0 + W1; ++i) ring[i] = LDN(i);
    __builtin_amdgcn_sched_barrier(0);
    pace(t_start, t_look + 5);
#pragma unroll
    for (int i = W0 + W1; i < W; ++i) ring[i] = LDN(i);
  }
  __builtin_amdgcn_sched_barrier(0);
  wg_barrier();
  const u32x4 qf = *(LAS u32x4*)(L + L_QF + (lane & 3) * 256 + q4 * 64 + ((lane >> 2) & 3) * 16);
  const unsigned pslow = __builtin_amdgcn_readfirstlane(*(LAS unsigned*)(L + L_FLEX));
  const bool give = slow && !pslow, take = !slow && pslow;
  const int ry = yl >> 1;
  const int swap = slow != pslow && (int)slow == 1 - (yl & 1);
  const int xr = xl ^ swap;
  const bool proj = ((yl & 1) ^ swap) == 0, ffn = !proj && ry < ((xr & 1) ? 12 : 16);
  const int hd = 4 * xr + (ry >> 2), nt = ry & 3;
  const int pf = 28 * (xr >> 1) + 16 * (xr & 1) + ry;
  if (proj) {
    const rsrc_t ro = mkrs(p.wo, HQ * DH * DM * 2);
    const unsigned vo = (unsigned)((hd * 128 + 16 * w + q4) * (DM * 2) + nt * 256 + c16 * 16);
#pragma unroll
    for (int j = 0; j < 4; ++j) dma16(ro, vo, 4u * j * (DM * 2), lbase + L_WO + w * 4096 + j * 1024);
  }
  constexpr int OOB = 0x40000000;
  if (give) { vka = OOB; vva = OOB; }
  if (!take) { vkb = OOB; vvb = OOB; }

  float T[18];
#pragma unroll
  for (int i = 0; i < NK; ++i) {
    const f32x4 d = mfma32(ring[i % W], qf, f32x4{0.f, 0.f, 0.f, 0.f});
    if (i + DEPTH >= W) ring[(i + DEPTH) % W] = LDN(i + DEPTH);
    const int x = (i & 7) >> 1, r = 2 * (i >> 3) + (i & 1);
    const float s = ((d[x] + dpp<ROR12>(d[(x + 1) & 3])) + dpp<ROR8>(d[(x + 2) & 3])) + dpp<ROR4>(d[(x + 3) & 3]);
    if (x == 0) T[r] = s;
    else if (x == 1) T[r] = asf(__builtin_amdgcn_update_dpp(asu(T[r]), asu(s), 0xE4, 0xf, 0x2, false));
    else if (x == 2) T[r] = asf(__builtin_amdgcn_update_dpp(asu(T[r]), asu(s), 0xE4, 0xf, 0x4, false));
    else T[r] = asf(__builtin_amdgcn_update_dpp(asu(T[r]), asu(s), 0xE4, 0xf, 0x8, false));
    if (i < 63 && (i & 7) == 7) __builtin_amdgcn_s_barrier();
    __builtin_amdgcn_sched_barrier(0);
  }
  unsigned P[9];
  float Mwg;
  {
    float mx = -INFINITY;
    const int bank = (lane >> 2) & 3;
#pragma unroll
    for (int t = 0; t < 9; ++t) {
      const unsigned u = cvtpk(T[2 * t], T[2 * t + 1]);
      const unsigned u2 = cvtpk(blo(u) * SCALE, bhi(u) * SCALE);
      T[2 * t] = blo(u2); T[2 * t + 1] = bhi(u2);
      if (t == 7 && give && bank == 3) { T[14] = -INFINITY; T[15] = -INFINITY; }
      if (t == 8 && !(take && bank == 0)) { T[16] = -INFINITY; T[17] = -INFINITY; }
      mx = fmaxf(mx, fmaxf(T[2 * t], T[2 * t + 1]));
    }
    mx = fmaxf(mx, dpp<ROR4>(mx)); mx = fmaxf(mx, dpp<ROR8>(mx)); mx = xrow_max(mx);
    if (lane < 4) *(LAS float*)(L + L_ML + (lane * 8 + w) * 4) = mx;
    wg_barrier();
    const f32x4 m0 = *(LAS f32x4*)(L + L_ML + (lane & 3) * 32), m1 = *(LAS f32x4*)(L + L_ML + (lane & 3) * 32 + 16);
    mx = fmaxf(fmaxf(fmaxf(m0[0], m0[1]), fmaxf(m0[2], m0[3])), fmaxf(fmaxf(m1[0], m1[1]), fmaxf(m1[2], m1[3])));
    float ls = 0.f;
#pragma unroll
    for (int t = 0; t < 9; ++t) {
      const float a = __builtin_amdgcn_exp2f((T[2 * t] - mx) * LOG2E), b = __builtin_amdgcn_exp2f((T[2 * t + 1] - mx) * LOG2E);
      ls += a + b;
      P[t] = cvtpk(a, b);
    }
    ls += dpp<ROR4>(ls); ls += dpp<ROR8>(ls);
    ls = xrow_sum(ls);
    if (lane < 4) *(LAS float*)(L + L_ML + 128 + (lane * 8 + w) * 4) = ls;
    Mwg = mx;
  }
  f32x4 acc[8];
#pragma unroll
  for (int j = 0; j < 8; ++j) acc[j] = f32x4{0.f, 0.f, 0.f, 0.f};
  const int mo = WS_ML + ((hd * 8 + w) * 4 + (lane & 3)) * 16;
  u32x4 mlg = {0u, 0u, 0u, 0u};
  unsigned xres = 0;
  float wt[4] = {0.f, 0.f, 0.f, 0.f};
#pragma unroll
  for (int t = 0; t < 8; ++t) {
    if (t == MLT && proj) {
      mlg = __builtin_amdgcn_raw_buffer_load_b128(rws, mo, 0, AUX_SC1);
      xres = *(const GAS unsigned*)(p.x + (xr * DM + 32 * ry + 2 * (tid & 15)) * 2);
    }
    if (t == 7 && proj) {
      if (!all_lanes(mlg.y == tag && mlg.w == tag)) {
        for (unsigned spins = 0;;) {
          SPIN_STEP();
          mlg = __builtin_amdgcn_raw_buffer_load_b128(rws, mo, 0, AUX_SC1);
          if (all_lanes(mlg.y == tag && mlg.w == tag)) break;
        }
      }
      float M = asf(mlg.x);
      M = fmaxf(M, dpp<XOR1>(M)); M = fmaxf(M, dpp<XOR2>(M));
      const float e = __builtin_amdgcn_exp2f((asf(mlg.x) - M) * LOG2E);
      float Ls = e * asf(mlg.z);
      Ls += dpp<XOR1>(Ls); Ls += dpp<XOR2>(Ls);
      const float wl = e * __builtin_amdgcn_rcpf(Ls);
      wt[0] = dpp<0x00>(wl); wt[1] = dpp<0x55>(wl); wt[2] = dpp<0xAA>(wl); wt[3] = dpp<0xFF>(wl);
      asm volatile("" : "+v"(wt[0]), "+v"(wt[1]), "+v"(wt[2]), "+v"(wt[3]), "+v"(xres));
    }
    u32x4 bf[8];
#pragma unroll
    for (int dw = 0; dw < 4; ++dw) {
      const int n0 = NK + 8 * t + 2 * dw, n1 = n0 + 1;
      const u32x4 a = ring[n0 % W], b = ring[n1 % W];
#pragma unroll
      for (int e = 0; e < 4; ++e) {
        bf[2 * e][dw] = __builtin_amdgcn_perm(b[e], a[e], 0x05040100u);
        bf[2 * e + 1][dw] = __builtin_amdgcn_perm(b[e], a[e], 0x07060302u);
      }
      if (n0 + DEPTH < 2 * NK) ring[(n0 + DEPTH) % W] = LDN(n0 + DEPTH);
      if (n1 + DEPTH < 2 * NK) ring[(n1 + DEPTH) % W] = LDN(n1 + DEPTH);
      __builtin_amdgcn_sched_barrier(0);
    }
    const u32x4 af = {P[t], dppu<ROR12>(P[t]), dppu<ROR8>(P[t]), dppu<ROR4>(P[t])};
#pragma unroll
    for (int j = 0; j < 8; ++j) acc[j] = mfma32(af, bf[j], acc[j]);
    if (t == 0) {
      wg_barrier();
      if (w == 0 && lane < 4) {
        const f32x4 l0 = *(LAS f32x4*)(L + L_ML + 128 + lane * 32), l1 = *(LAS f32x4*)(L + L_ML + 128 + lane * 32 + 16);
        const float Ls = ((l0[0] + l0[1]) + (l0[2] + l0[3])) + ((l1[0] + l1[1]) + (l1[2] + l1[3]));
        __builtin_amdgcn_raw_buffer_store_b128(u32x4{asu(Mwg), tag, asu(Ls), tag}, rws, WS_ML + (((ag * 4 + lane) * 8 + ab) * 4 + as) * 16, 0, AUX_SC1);
      }
    } else if (t < 7) __builtin_amdgcn_s_barrier();
    __builtin_amdgcn_sched_barrier(0);
  }
  if (take) {
    const u32x4 a = ring[(2 * NK - 2) % W], b = ring[(2 * NK - 1) % W];
    const u32x2 af = {P[8], 0u};
#pragma unroll
    for (int e = 0; e < 4; ++e) {
      acc[2 * e] = mfma16(af, u32x2{__builtin_amdgcn_perm(b[e], a[e], 0x05040100u), 0u}, acc[2 * e]);
      acc[2 * e + 1] = mfma16(af, u32x2{__builtin_amdgcn_perm(b[e], a[e], 0x07060302u), 0u}, acc[2 * e + 1]);
    }
  }
  if (lane < 16) {
#pragma unroll
    for (int j = 0; j < 8; ++j) *(LAS f32x4*)(L + L_WQ + w * 2048 + j * 256 + lane * 16) = acc[j];
  }
  asm volatile("s_waitcnt vmcnt(0)" ::: "memory");
  wg_barrier();
  const unsigned t_merge = (unsigned)__builtin_amdgcn_s_memrealtime();
  if (w < 4) {
    float o0 = 0.f, o1 = 0.f;
#pragma unroll
    for (int ww = 0; ww < 8; ++ww) {
      LAS float* src = (LAS float*)(L + L_WQ + ww * 2048 + w * 512 + c16 * 16 + q4 * 4);
      o0 += src[0]; o1 += src[64];
    }
    __builtin_amdgcn_raw_buffer_store_b128(u32x4{asu(o0), tag, asu(o1), tag}, rws, WS_O + ((ag * 8 + ab) * 4 + as) * 4096 + w * 1024 + lane * 16, 0, AUX_SC1);
  }
  if (proj) {
    u32x4 wof[4];
#pragma unroll
    for (int t = 0; t < 4; ++t) {
      LAS unsigned char* pa = L + L_WO + (32 * t + 8 * q4 + (c16 >> 2)) * 256 + w * 32 + (c16 & 3) * 8;
      const u32x2 a0 = trread(pa), a1 = trread(pa + 4 * 256);
      wof[t] = u32x4{a0.x, a0.y, a1.x, a1.y};
    }
    unsigned hres = 0;
    const int rr = xr * 16 + ry;
    {
      const int oo = WS_O + ((xr * 8 + w) * 4) * 4096 + q4 * 1024 + (hd & 3) * 256 + c16 * 16;
      u32x4 og[4];
      pace(t_merge, T1);
      for (unsigned spins = 0;;) {
        bool ok = true;
#pragma unroll
        for (int s = 0; s < 4; ++s) og[s] = __builtin_amdgcn_raw_buffer_load_b128(rws, oo + s * 4096, 0, AUX_SC1);
#pragma unroll
        for (int s = 0; s < 4; ++s) ok = ok && og[s].y == tag && og[s].w == tag;
        if (failed || all_lanes(ok)) break;
        SPIN_STEP();
      }
      float O0 = 0.f, O1 = 0.f;
#pragma unroll
      for (int s = 0; s < 4; ++s) { O0 = fmaf(wt[s], asf(og[s].x), O0); O1 = fmaf(wt[s], asf(og[s].z), O1); }
      *(LAS unsigned*)(L + L_OB + w * OB_STRIDE + c16 * 16 + q4 * 4) = cvtpk(O0, O1);
      wg_barrier();
      f32x4 y = {0.f, 0.f, 0.f, 0.f};
#pragma unroll
      for (int t = 0; t < 4; ++t) y = mfma32(wof[t], *(LAS u32x4*)(L + L_OB + (c16 & 7) * OB_STRIDE + (32 * t + 8 * q4) * 2), y);
      const int r = (c16 & 7) * 16 + nt * 4 + (w >> 1);
      __builtin_amdgcn_raw_buffer_store_b128(line_piece(y, tag), rws, WS_Y + (r * 32 + hd) * 256 + (w & 1) * 128 + q4 * 32 + (c16 >> 3) * 16, 0, AUX_SC1);
    }
    {
      const int yo = WS_Y + rr * 8192 + tid * 16;
      u32x4 g;
      __builtin_amdgcn_s_sleep(DLY2);
      for (unsigned spins = 0;;) {
        g = __builtin_amdgcn_raw_buffer_load_b128(rws, yo, 0, AUX_SC1);
        if (failed || all_lanes(g.y == tag && g.w == tag)) break;
        SPIN_STEP();
      }
      const float a = xrow_sum(asf(g.x)), b = xrow_sum(asf(g.z));
      if (lane < 16) *(LAS f32x2*)(L + L_RED + (w * 16 + lane) * 8) = f32x2{a, b};
      wg_barrier();
      if (tid < 16) {
        float s0 = 0.f, s1 = 0.f;
#pragma unroll
        for (int ww = 0; ww < 8; ++ww) { const f32x2 v = *(LAS f32x2*)(L + L_RED + (ww * 16 + tid) * 8); s0 += v.x; s1 += v.y; }
        const unsigned at = cvtpk(s0, s1);
        hres = cvtpk(blo(xres) + blo(at), bhi(xres) + bhi(at));
        __builtin_amdgcn_raw_buffer_store_b64(u32x2{hres, tag}, rws, WS_H + (xr * 256 + 16 * ry + tid) * 8, 0, AUX_SC1);
      }
    }
    {
      const int dof = WS_D + rr * (NFFN * 128) + tid * 16;
      const bool two = tid < (NFFN - 64) * 8;
      u32x4 g, g2 = {0u, tagA, 0u, tagB};
      __builtin_amdgcn_s_sleep(DLY4);
      for (unsigned spins = 0;;) {
        g = __builtin_amdgcn_raw_buffer_load_b128(rws, dof, 0, AUX_SC1);
        if (two) g2 = __builtin_amdgcn_raw_buffer_load_b128(rws, dof + 8192, 0, AUX_SC1);
        if (failed || all_lanes(tags4(g, tagA, tagB) && tags4(g2, tagA, tagB))) break;
        SPIN_STEP();
      }
      f32x4 a = unpack4(g) + unpack4(g2);
#pragma unroll
      for (int e = 0; e < 4; ++e) { a[e] += dpp<ROR8>(a[e]); a[e] = xrow_sum(a[e]); }
      if (lane < 8) *(LAS f32x4*)(L + L_RED + 1024 + (w * 8 + lane) * 16) = a;
      wg_barrier();
      if (tid < 16) {
        float s0 = 0.f, s1 = 0.f;
#pragma unroll
        for (int ww = 0; ww < 8; ++ww) { const f32x2 v = *(LAS f32x2*)(L + L_RED + 1024 + (ww * 8 + (tid >> 1)) * 16 + (tid & 1) * 8); s0 += v.x; s1 += v.y; }
        const unsigned f = cvtpk(s0, s1);
        __builtin_amdgcn_raw_buffer_store_b32(cvtpk(blo(hres) + blo(f), bhi(hres) + bhi(f)), mkrs(p.out, NB * DM * 2), (xr * DM + 32 * ry + 2 * tid) * 2, 0, AUX_SC1);
      }
      if (rr == 0 && tid == 0) __hip_atomic_store((GAS unsigned*)(p.ws + WS_GEN), tag, __ATOMIC_RELAXED, __HIP_MEMORY_SCOPE_AGENT);
    }
  } else if (ffn) {
    {
      const rsrc_t rg = mkrs(p.wg, DM * FF * 2), ru = mkrs(p.wu, DM * FF * 2), rd = mkrs(p.wd, FF * DM * 2);
      const unsigned vd = (unsigned)((pf * 16 + 2 * w) * (DM * 2) + lane * 16);
      const unsigned vg = (unsigned)((64 * w + (lane >> 1)) * (FF * 2) + pf * 32 + (lane & 1) * 16);
      dma16(rg, vg, 0u, lbase + L_WG + w * 2048);
      dma16(rg, vg, 32u * (FF * 2), lbase + L_WG + w * 2048 + 1024);
      dma16(ru, vg, 0u, lbase + L_WU + w * 2048);
      dma16(ru, vg, 32u * (FF * 2), lbase + L_WU + w * 2048 + 1024);
      dma16(rd, vd, 0u, lbase + L_WD + w * 2048);
      dma16(rd, vd, (unsigned)(DM * 2), lbase + L_WD + w * 2048 + 1024);
    }
    asm volatile("s_waitcnt vmcnt(2)" ::: "memory");
    u32x4 wgf[2], wuf[2];
    u32x2 wdf[8];
#pragma unroll
    for (int t = 0; t < 2; ++t) {
      LAS unsigned char* pg = L + L_WG + (64 * w + 32 * t + 8 * q4 + (c16 >> 2)) * 32 + (c16 & 3) * 8;
      const u32x2 g0 = trread(pg), g1 = trread(pg + 128), u0 = trread(pg + (L_WU - L_WG)), u1 = trread(pg + (L_WU - L_WG) + 128);
      wgf[t] = u32x4{g0.x, g0.y, g1.x, g1.y};
      wuf[t] = u32x4{u0.x, u0.y, u1.x, u1.y};
    }
    const u32x4 ones = {0x3f803f80u, 0x3f803f80u, 0x3f803f80u, 0x3f803f80u};
    f32x4 cs = {0.f, 0.f, 0.f, 0.f};
    {
      f32x4 cg = {0.f, 0.f, 0.f, 0.f}, cu = {0.f, 0.f, 0.f, 0.f};
#pragma unroll
      for (int t = 0; t < 2; ++t) { cg = mfma32(wgf[t], ones, cg); cu = mfma32(wuf[t], ones, cu); }
      *(LAS f32x4*)(L + L_XN + w * 1024 + lane * 16) = c16 < 8 ? cg : cu;
      asm volatile("s_waitcnt vmcnt(0)" ::: "memory");
      wg_barrier();
#pragma unroll
      for (int ww = 0; ww < 8; ++ww) cs += *(LAS f32x4*)(L + L_XN + ww * 1024 + lane * 16);
      asm volatile("" : "+v"(cs));
#pragma unroll
      for (int j = 0; j < 8; ++j) wdf[j] = trread(L + L_WD + (4 * q4 + (c16 >> 2)) * 1024 + (128 * (w & 3) + 16 * j) * 2 + (c16 & 3) * 8);
#pragma unroll
      for (int j = 0; j < 8; ++j) asm volatile("" : "+v"(wdf[j]));
    }
    const int ho = WS_H + (c16 & 7) * 2048 + (32 * w + 4 * q4 + 2 * (c16 >> 3)) * 8;
    const bool diag = c16 < 8 && q4 == (c16 >> 2);
    u32x4 g0, g1;
    __builtin_amdgcn_s_sleep(DLY3);
    for (unsigned spins = 0;;) {
      g0 = __builtin_amdgcn_raw_buffer_load_b128(rws, ho, 0, AUX_SC1); g1 = __builtin_amdgcn_raw_buffer_load_b128(rws, ho + 128, 0, AUX_SC1);
      if (all_lanes(g0.y == tag && g0.w == tag && g1.y == tag && g1.w == tag)) break;
      SPIN_STEP();
    }
    {
      const u32x4 b0 = {g0.x, g0.z, dppu<ROR8>(g0.x), dppu<ROR8>(g0.z)}, b1 = {g1.x, g1.z, dppu<ROR8>(g1.x), dppu<ROR8>(g1.z)};
      const f32x4 z4 = {0.f, 0.f, 0.f, 0.f};
      f32x4 ga = mfma32(wgf[0], b0, z4), ua = mfma32(wuf[0], b0, z4);
      f32x4 s1 = mfma32(ones, b0, z4), s2 = mfma32(b0, b0, z4);
      ga = mfma32(wgf[1], b1, ga); ua = mfma32(wuf[1], b1, ua);
      s1 = mfma32(ones, b1, s1); s2 = mfma32(b1, b1, s2);
      f32x4 gu;
#pragma unroll
      for (int j = 0; j < 4; ++j) gu[j] = asf(__builtin_amdgcn_update_dpp(asu(ga[j]), asu(ua[j]), ROR8, 0xf, 0xc, false));
      *(LAS f32x4*)(L + L_WQ + 8192 + w * 1024 + lane * 16) = gu;
      const float sq = (c16 & 2) ? ((c16 & 1) ? s2[3] : s2[2]) : ((c16 & 1) ? s2[1] : s2[0]);
      if (diag) *(LAS f32x2*)(L + L_RED + (c16 * 8 + w) * 8) = f32x2{s1[0], sq};
    }
    wg_barrier();
    if (w < 4) {
      f32x4 X = {0.f, 0.f, 0.f, 0.f};
#pragma unroll
      for (int ww = 0; ww < 8; ++ww) X += *(LAS f32x4*)(L + L_WQ + 8192 + ww * 1024 + lane * 16);
      f32x4 sv = {0.f, 0.f, 0.f, 0.f};
#pragma unroll
      for (int ww = 0; ww < 4; ++ww) sv += *(LAS f32x4*)(L + L_RED + (c16 & 7) * 64 + ww * 16);
      const float mean = (sv[0] + sv[2]) * (1.0f / DM);
      const float var = bfr(fmaxf((sv[1] + sv[3]) * (1.0f / DM) - mean * mean, 0.f));
      const float mb = bfr(mean);
      const float rd = __builtin_amdgcn_rcpf(bfr(__builtin_amdgcn_sqrtf(bfr(var + EPS))));
      const unsigned p01 = cvtpk((X[0] - mb * cs[0]) * rd, (X[1] - mb * cs[1]) * rd), p23 = cvtpk((X[2] - mb * cs[2]) * rd, (X[3] - mb * cs[3]) * rd);
      const unsigned gp = __builtin_amdgcn_update_dpp(p01, p23, ROR8, 0xf, 0xc, false), up = __builtin_amdgcn_update_dpp(p23, p01, ROR8, 0xf, 0x3, false);
      float act[2];
#pragma unroll
      for (int j = 0; j < 2; ++j) {
        const float gv = j ? bhi(gp) : blo(gp), uv = j ? bhi(up) : blo(up);
        const float e = __builtin_amdgcn_exp2f(gv * fmaf(gv * gv, -2.0f * LOG2E * 0.7978845608028654f * 0.044715f, -2.0f * LOG2E * 0.7978845608028654f));
        act[j] = bfr(gv * __builtin_amdgcn_rcpf(1.0f + e)) * uv;
      }
      const unsigned ap = cvtpk(act[0], act[1]);
      const u32x2 ab2 = {ap, dppu<ROR8>(ap)};
      f32x4 d[8];
#pragma unroll
      for (int j = 0; j < 8; ++j) d[j] = mfma16(wdf[j], ab2, f32x4{0.f, 0.f, 0.f, 0.f});
      __builtin_amdgcn_sched_barrier(0);
      const int dof = WS_D + (((c16 & 7) * 16 + w * 4) * NFFN + pf) * 128 + (c16 >> 3) * 64 + q4 * 16;
#pragma unroll
      for (int jp = 0; jp < 4; ++jp) {
        float v[4];
#pragma unroll
        for (int e = 0; e < 4; ++e) v[e] = asf(__builtin_amdgcn_update_dpp(asu(d[2 * jp][e]), asu(d[2 * jp + 1][e]), ROR8, 0xf, 0xc, false));
        __builtin_amdgcn_raw_buffer_store_b128(pack4(v[0], v[1], v[2], v[3], tagA, tagB), rws, dof + jp * (NFFN * 128), 0, AUX_SC1);
      }
    }
  }
}

}

extern "C" void kernel_launch(void* const* d_in, const int* in_sizes, int n_in, void* d_out, int out_size, void* d_ws, size_t ws_size,
                              hipStream_t stream) {
  Params p;
  p.x = (const unsigned char*)d_in[0];
  p.k = (const unsigned char*)d_in[1];
  p.v = (const unsigned char*)d_in[2];
  p.wq = (const unsigned char*)d_in[3];
  p.wo = (const unsigned char*)d_in[4];
  p.wg = (const unsigned char*)d_in[6];
  p.wu = (const unsigned char*)d_in[7];
  p.wd = (const unsigned char*)d_in[8];
  p.out = (unsigned char*)d_out;
  p.ws = (unsigned char*)d_ws;
  fused_decode<<<NWG, NTHR, 0, stream>>>(p);
}
```
